```python
import torch

def _softmax(x, axis=-1):
    return torch.softmax(x, dim=axis)

def _gelu(x):
    return torch.nn.functional.gelu(x, approximate='tanh')

def _silu(x):
    return torch.nn.functional.silu(x)

def _sigmoid(x):
    return torch.sigmoid(x)

def _layernorm(x, axis=-1, eps=1e-05):
    m = x.mean(dim=axis, keepdim=True)
    v = x.var(dim=axis, keepdim=True, unbiased=False)
    return (x - m) / torch.sqrt(v + eps)

def _scatter_add(tgt, idx, src, axis=0):
    return tgt.clone().index_add_(axis, idx, src)

def _segment_sum(src, seg, n):
    out = torch.zeros((n,) + src.shape[1:], dtype=src.dtype)
    return out.index_add_(0, seg, src)

def _pad(x, pw):
    flat = tuple((v for pair in reversed(pw) for v in pair))
    return torch.nn.functional.pad(x, flat)
kernel_name = 'adaLN_cond__memory_aug_recurrent_mem_tr__mixer_channel_mlp__prompt'

def setup_inputs():

    _w = lambda a: (lambda t: t.to(torch.bfloat16) if t.is_floating_point() else t)(torch.as_tensor(a))

    def _A_setup():
        B, S, Dm = (1, 2048, 640)
        H, Dh = (32, 192)
        M, seg = (8, 32)
        seg = min(seg, S)
        S = S // seg * seg
        return {'x': _w((lambda *s: torch.randn(*s, dtype=torch.bfloat16) * 0.05)(B, S, Dm)), 'mem0': _w((lambda *s: torch.randn(*s, dtype=torch.bfloat16) * 0.4)(B, M, Dm)), 'Wq': _w((lambda *s: torch.randn(*s, dtype=torch.bfloat16) * 1.6)(Dm, H, Dh) * Dm ** (-0.5)), 'Wk': _w((lambda *s: torch.randn(*s, dtype=torch.bfloat16) * 1.6)(Dm, H, Dh) * Dm ** (-0.5)), 'Wv': _w((lambda *s: torch.randn(*s, dtype=torch.bfloat16) * 0.4)(Dm, H, Dh) * Dm ** (-0.5)), 'Wo': _w((lambda *s: torch.randn(*s, dtype=torch.bfloat16) * 0.4)(H, Dh, Dm) * (H * Dh) ** (-0.5)), 'seg_len': seg}
    _A_in = _A_setup()

    def _B_setup():
        B, S, D, F = (1, 2048, 640, 1536)
        return {'x': _w((lambda *s: torch.randn(*s, dtype=torch.bfloat16) * 0.05)(B, S, D)), 'w1': _w((lambda *s: torch.randn(*s, dtype=torch.bfloat16) * 1.6)(D, F) * D ** (-0.5)), 'b1': _w((lambda *s: torch.randn(*s, dtype=torch.bfloat16) * 0.4)(F)), 'w2': _w((lambda *s: torch.randn(*s, dtype=torch.bfloat16) * 0.1)(F, D) * F ** (-0.5)), 'b2': _w((lambda *s: torch.randn(*s, dtype=torch.bfloat16) * 0.1)(D))}
    _B_in = _B_setup()
    return {'A_x': _A_in['x'], 'A_mem0': _A_in['mem0'], 'A_Wq': _A_in['Wq'], 'A_Wk': _A_in['Wk'], 'A_Wv': _A_in['Wv'], 'A_Wo': _A_in['Wo'], 'A_seg_len': _A_in['seg_len'], 'B_x': _B_in['x'], 'B_w1': _B_in['w1'], 'B_b1': _B_in['b1'], 'B_w2': _B_in['w2'], 'B_b2': _B_in['b2'], 'c': _w((lambda *s: torch.randn(*s, dtype=torch.bfloat16) * 0.1)(1, 640) * 8.0), 'w_mod': _w((lambda *s: torch.randn(*s, dtype=torch.bfloat16) * 0.1)(640, 6 * 640) * 640 ** (-0.5) * 8.0)}

def reference(A_x, A_mem0, A_Wq, A_Wk, A_Wv, A_Wo, A_seg_len, B_x, B_w1, B_b1, B_w2, B_b2, c, w_mod):

    def _A_ref(x, mem0, Wq, Wk, Wv, Wo, seg_len):
        B, S, Dm = x.shape
        H, Dh = (Wq.shape[1], Wq.shape[2])
        M = mem0.shape[1]
        scale = Dh ** (-0.5)
        n_seg = S // seg_len
        L = M + seg_len
        idx = torch.arange(L)
        mask = (idx[:, None] < M) | (idx[:, None] >= idx[None, :])
        mem = mem0
        outs = []
        for s in range(n_seg):
            xs = x[:, s * seg_len:(s + 1) * seg_len, :].clone()
            h = torch.concatenate([mem, xs], axis=1)
            q = torch.einsum('btd,dhe->bhte', h, Wq)
            k = torch.einsum('btd,dhe->bhte', h, Wk)
            v = torch.einsum('btd,dhe->bhte', h, Wv)
            scores = torch.einsum('bhqe,bhke->bhqk', q, k) * scale
            scores = torch.where(mask[None, None], scores, -1000000000.0)
            p = _softmax(scores, axis=-1)
            o = torch.einsum('bhqk,bhke->bhqe', p, v)
            o = torch.einsum('bhte,hed->btd', o, Wo)
            mem = o[:, :M, :].clone()
            outs.append(o[:, M:, :])
        return torch.concatenate(outs, axis=1)

    def _B_ref(x, w1, b1, w2, b2):
        y = _layernorm(x, axis=-1)
        h = _gelu(torch.einsum('bsd,df->bsf', y, w1) + b1)
        return torch.einsum('bsf,fd->bsd', h, w2) + b2
    x = A_x
    mod = (_silu(c) @ w_mod).reshape(c.shape[0], 6, 1, -1)
    sa, ca, ga, sb, cb, gb = (mod[:, 0].clone(), mod[:, 1].clone(), mod[:, 2].clone(), mod[:, 3].clone(), mod[:, 4].clone(), mod[:, 5].clone())
    h = _layernorm(x, axis=-1) * (1 + ca) + sa
    x = x + ga * _A_ref(h, A_mem0, A_Wq, A_Wk, A_Wv, A_Wo, A_seg_len)
    h = _layernorm(x, axis=-1) * (1 + cb) + sb
    x = x + gb * _B_ref(h, B_w1, B_b1, B_w2, B_b2)
    return x
```

```cpp
#include <hip/hip_runtime.h>
#include <hip/hip_bf16.h>
#include <stdint.h>

namespace {

typedef __attribute__((ext_vector_type(2))) __bf16 bf16x2;
typedef __attribute__((ext_vector_type(4))) __bf16 bf16x4;
typedef __attribute__((ext_vector_type(8))) __bf16 bf16x8;
typedef __attribute__((ext_vector_type(16))) __bf16 bf16x16;
typedef __attribute__((ext_vector_type(4))) short s16x4;
typedef __attribute__((ext_vector_type(2))) float f32x2;
typedef __attribute__((ext_vector_type(4))) float f32x4;
typedef __attribute__((ext_vector_type(2))) unsigned u32x2;
typedef __attribute__((ext_vector_type(4))) unsigned u32x4;
typedef __attribute__((ext_vector_type(8))) unsigned u32x8;
typedef unsigned short u16;
typedef __amdgpu_buffer_rsrc_t rsrc_t;
#define GAS __attribute__((address_space(1)))
#define LAS __attribute__((address_space(3)))
#define DEV __device__ __forceinline__
#define RLX_AGENT __ATOMIC_RELAXED, __HIP_MEMORY_SCOPE_AGENT

constexpr int NTOK = 2048, DM = 640, NH = 32, DH = 192, NSEG = 64, FF = 1536, NBC = 80;
constexpr int LDW = NH * DH;

constexpr unsigned OFF_CTL = 0;
constexpr unsigned OFF_MOD = 65536;
constexpr unsigned OFF_MBX = 1u << 20;
constexpr unsigned OFF_HF  = 5u << 20;
constexpr unsigned OFF_AO  = 8u << 20;
constexpr unsigned OFF_X1  = 11u << 20;
constexpr unsigned OFF_YF  = 14u << 20;
constexpr unsigned OFF_HID = 17u << 20;
constexpr unsigned OFF_W1F = 24u << 20;
constexpr unsigned OFF_W2F = 26u << 20;
constexpr unsigned OFF_PX  = 28u << 20;
constexpr unsigned OFF_KM  = 34u << 20;
constexpr unsigned OFF_WVO = 47u << 20;
constexpr unsigned OFF_KX  = 72u << 20;
constexpr unsigned OFF_VX  = 96u << 20;
constexpr unsigned OFF_U   = 120u << 20;
constexpr unsigned OFF_DBG = 200u << 20;
constexpr unsigned WS_USED = 208u << 20;

constexpr int CT_TICKET = 0;
constexpr int CT_SPARE = 512, CT_PAR = 576, CT_TMO = 640;
constexpr int CT_PROG = 1024;
enum { PG_K = 0, PG_V = 1, PG_U = 2, PG_KM = 3, PG_PX = 4 };
constexpr int CT_WORDS = CT_PROG + 5 * 32 * 32;

constexpr int NSLOT = 4;
constexpr int MB_P = 0, MB_MA = 20480, MB_MB = 30720, MB_END = 40960;
constexpr int MB_SLOT = 40960;
constexpr unsigned SENT = 0xFFFFFFFFu;
constexpr unsigned SPIN_CAP = 200000u;
constexpr int LAG = 3;

DEV int l2p(int x) { return ((x >> 13) << 14) + (x & 8191); }
DEV int tokslot(int tt, int r) { return 8 * (r >> 2) + 4 * tt + (r & 3); }

DEV float bf2f(u16 v) { return __builtin_bit_cast(float, (unsigned)v << 16); }
DEV float lo2f(unsigned v) { return __builtin_bit_cast(float, v << 16); }
DEV float hi2f(unsigned v) { return __builtin_bit_cast(float, v & 0xffff0000u); }
DEV unsigned pk2(float a, float b) { f32x2 x = {a, b}; return __builtin_bit_cast(unsigned, __builtin_convertvector(x, bf16x2)); }
DEV float rbf(float a) { return lo2f(pk2(a, a)); }
DEV unsigned nosent(unsigned v) { return v == SENT ? 0x7FFF7FFFu : v; }

DEV u32x4 ld16(rsrc_t r, int v, int s) { return __builtin_amdgcn_raw_buffer_load_b128(r, v, s, 0); }
DEV u32x4 ld16c(rsrc_t r, int v, int s) { return __builtin_amdgcn_raw_buffer_load_b128(r, v, s, 16); }
DEV u32x2 ld8c(rsrc_t r, int v, int s) { return __builtin_amdgcn_raw_buffer_load_b64(r, v, s, 16); }
DEV unsigned ld4c(rsrc_t r, int v, int s) { return __builtin_amdgcn_raw_buffer_load_b32(r, v, s, 16); }
DEV void st16(rsrc_t r, int v, int s, u32x4 x) { __builtin_amdgcn_raw_buffer_store_b128(x, r, v, s, 0); }
DEV void st8(rsrc_t r, int v, int s, u32x2 x) { __builtin_amdgcn_raw_buffer_store_b64(x, r, v, s, 0); }
DEV void st16c(rsrc_t r, int v, int s, u32x4 x) { __builtin_amdgcn_raw_buffer_store_b128(x, r, v, s, 16); }
DEV void st8c(rsrc_t r, int v, int s, u32x2 x) { __builtin_amdgcn_raw_buffer_store_b64(x, r, v, s, 16); }
DEV void st4c(rsrc_t r, int v, int s, unsigned x) { __builtin_amdgcn_raw_buffer_store_b32(x, r, v, s, 16); }
DEV rsrc_t mkrsrc(const void* p, unsigned bytes) { return __builtin_amdgcn_make_buffer_rsrc((void*)p, 0, (int)bytes, 0x00020000); }
DEV void dma16(rsrc_t r, int voff, int soff, unsigned ldsaddr) {
  asm volatile("s_nop 4\n\ts_mov_b32 m0, %0\n\ts_nop 0\n\tbuffer_load_dwordx4 %1, %2, %3 offen lds"
               :: "s"(ldsaddr), "v"(voff), "s"(r), "s"(soff) : "memory");
}
template <int N> DEV void waitvm() { asm volatile("s_waitcnt vmcnt(%0)" :: "i"(N) : "memory"); }
DEV void waitlds() { asm volatile("s_waitcnt lgkmcnt(0)" ::: "memory"); }
DEV void bar() { asm volatile("s_waitcnt lgkmcnt(0)\n\ts_barrier" ::: "memory"); }

DEV unsigned umax3(unsigned a, unsigned b, unsigned c) { return max(max(a, b), c); }
DEV unsigned mx4(u32x4 v) { return max(umax3(v.x, v.y, v.z), v.w); }
DEV f32x4 mfma32(u32x4 a, u32x4 b, f32x4 c) {
  return __builtin_amdgcn_mfma_f32_16x16x32_bf16(__builtin_bit_cast(bf16x8, a), __builtin_bit_cast(bf16x8, b), c, 0, 0, 0); }
DEV f32x4 mfma16(u32x2 a, u32x2 b, f32x4 c) {
  return __builtin_amdgcn_mfma_f32_16x16x16bf16_1k(__builtin_bit_cast(s16x4, a), __builtin_bit_cast(s16x4, b), c, 0, 0, 0); }
DEV f32x4 smfma(u32x4 a, u32x8 b, f32x4 c, int idx) {
  return __builtin_amdgcn_smfmac_f32_16x16x64_bf16(__builtin_bit_cast(bf16x8, a), __builtin_bit_cast(bf16x16, b), c, idx, 0, 0); }
DEV u32x2 trread(LAS unsigned char* p) { return __builtin_bit_cast(u32x2, __builtin_amdgcn_ds_read_tr16_b64_v4bf16((LAS bf16x4*)p)); }
DEV u32x4 cat(u32x2 a, u32x2 b) { return u32x4{a.x, a.y, b.x, b.y}; }
DEV u32x8 cat8(u32x4 a, u32x4 b) { return u32x8{a.x, a.y, a.z, a.w, b.x, b.y, b.z, b.w}; }
DEV u32x2 pk4(f32x4 v) { return u32x2{pk2(v[0], v[1]), pk2(v[2], v[3])}; }
template <int C> DEV float dppf(float v) {
  return __builtin_bit_cast(float, __builtin_amdgcn_update_dpp(0, __builtin_bit_cast(int, v), C, 0xf, 0xf, true)); }
constexpr int DPP_X1 = 0xB1, DPP_X2 = 0x4E, DPP_HM = 0x141, DPP_RM = 0x140;
DEV unsigned fbits(float v) { return __builtin_bit_cast(unsigned, v); }
DEV float bitsf(unsigned v) { return __builtin_bit_cast(float, v); }
DEV float fold32(float a, float b) {
  auto r = __builtin_amdgcn_permlane32_swap(fbits(a), fbits(b), false, false);
  return bitsf(r[0]) + bitsf(r[1]); }
DEV f32x4 fold4(f32x4 a, f32x4 b) { return f32x4{fold32(a[0], b[0]), fold32(a[1], b[1]), fold32(a[2], b[2]), fold32(a[3], b[3])}; }
DEV unsigned xcc_id() { return (unsigned)__builtin_amdgcn_s_getreg(20 | (3 << 11)) & 7u; }
DEV unsigned mbx_base(const unsigned char* ws) {
  const uint64_t a = (uint64_t)ws;
  return (unsigned)(((a + OFF_MBX + 0x1FFFFFull) & ~0x1FFFFFull) - a); }

constexpr int PRE_WVO = 256, PRE_MOD = 60, PRE_W1 = 60, PRE_W2 = 60;
constexpr int PRE_GRID = PRE_WVO + PRE_MOD + PRE_W1 + PRE_W2 + 1;

DEV void retile(const u16* W, int ncols, int KS, int ks, int n0, int nw, int perm, unsigned char* dst, LAS unsigned char* lds) {
  const int tid = threadIdx.x, lane = tid & 63, wave = tid >> 6;
  const int cpr = nw / 8;
  const int rs = nw * 2 + 16;
  for (int p = tid; p < 32 * cpr; p += 256) {
    const int r = p / cpr, c = p % cpr;
    *(LAS u32x4*)(lds + r * rs + c * 16) = *(const u32x4*)(W + (size_t)(32 * ks + r) * ncols + n0 + c * 8);
  }
  __syncthreads();
  const int g = lane >> 4, q = (lane & 15) >> 2, pp = lane & 3;
  for (int t = wave; t < nw / 16; t += 4) {
    const int r0 = perm ? 4 * g + q : 8 * g + q, r1 = perm ? 16 + 4 * g + q : 8 * g + 4 + q;
    const u32x2 a = trread(lds + r0 * rs + t * 32 + pp * 8), b = trread(lds + r1 * rs + t * 32 + pp * 8);
    *(u32x4*)(dst + ((size_t)(n0 / 16 + t) * KS + ks) * 1024 + lane * 16) = cat(a, b);
  }
}

__global__ void __launch_bounds__(256) k_pre(const u16* __restrict__ mem0, const u16* __restrict__ Wv, const u16* __restrict__ Wo,
                                             const u16* __restrict__ w1, const u16* __restrict__ w2,
                                             const u16* __restrict__ cvec, const u16* __restrict__ wmod, unsigned char* __restrict__ ws) {
  __shared__ __attribute__((aligned(16))) unsigned char smem[33280];
  LAS unsigned char* lds = (LAS unsigned char*)smem;
  const int tid = threadIdx.x, lane = tid & 63, wave = tid >> 6;
  int b = blockIdx.x;
  if (b < PRE_WVO) {
    const int h = (b & 7) * 4 + ((b >> 3) & 3), cb = b >> 5;
    for (int p = tid; p < 1920; p += 256) {
      const int r = p / 10, c = p % 10;
      *(LAS u32x4*)(lds + r * 160 + c * 16) = *(const u32x4*)(Wo + (size_t)(h * DH + r) * DM + cb * 80 + c * 8);
    }
    __syncthreads();
    const int g = lane >> 4, n = lane & 15, q = n >> 2, pp = lane & 3;
    u32x4 Bf[6][5];
#pragma unroll
    for (int ks = 0; ks < 6; ++ks)
#pragma unroll
      for (int t = 0; t < 5; ++t)
        Bf[ks][t] = cat(trread(lds + (32 * ks + 8 * g + q) * 160 + t * 32 + pp * 8), trread(lds + (32 * ks + 8 * g + 4 + q) * 160 + t * 32 + pp * 8));
    for (int kd = wave; kd < 20; kd += 4) {
      u32x4 Af[2][6];
#pragma unroll
      for (int tt = 0; tt < 2; ++tt)
#pragma unroll
        for (int ks = 0; ks < 6; ++ks)
          Af[tt][ks] = *(const u32x4*)(Wv + (size_t)(32 * kd + tokslot(tt, n)) * LDW + h * DH + 32 * ks + 8 * g);
#pragma unroll
      for (int t = 0; t < 5; ++t) {
        f32x4 a0 = {0, 0, 0, 0}, a1 = {0, 0, 0, 0};
#pragma unroll
        for (int ks = 0; ks < 6; ++ks) { a0 = mfma32(Af[0][ks], Bf[ks][t], a0); a1 = mfma32(Af[1][ks], Bf[ks][t], a1); }
        const int j = 2 * (5 * cb + t) + ((n >> 1) & 1), c = 2 * (n >> 2) + (n & 1);
        const unsigned off = ((((unsigned)(j * 16 + (h >> 1)) * 20 + kd) * 64) + g * 16 + (h & 1) * 8 + c) * 16;
        *(u32x4*)(ws + OFF_WVO + off) = cat(pk4(a0), pk4(a1));
      }
    }
    return;
  }
  b -= PRE_WVO;
  if (b < PRE_MOD) {
    LAS float* sc = (LAS float*)lds;
    LAS float* red = (LAS float*)(lds + 4096);
    for (int i = tid; i < DM; i += 256) { const float v = bf2f(cvec[i]); sc[i] = rbf(v / (1.f + __expf(-v))); }
    __syncthreads();
    const int cg = tid & 7, rg = tid >> 3;
    float acc[8] = {0, 0, 0, 0, 0, 0, 0, 0};
#pragma unroll 4
    for (int i = 0; i < 20; ++i) {
      const int r = rg + 32 * i;
      const u32x4 w = *(const u32x4*)(wmod + (size_t)r * 3840 + b * 64 + cg * 8);
      const float s = sc[r];
#pragma unroll
      for (int k = 0; k < 4; ++k) { acc[2 * k] += s * lo2f(w[k]); acc[2 * k + 1] += s * hi2f(w[k]); }
    }
#pragma unroll
    for (int k = 0; k < 8; ++k) red[rg * 64 + cg * 8 + k] = acc[k];
    __syncthreads();
    if (tid < 64) { float s = 0.f; for (int r = 0; r < 32; ++r) s += red[r * 64 + tid]; ((float*)(ws + OFF_MOD))[b * 64 + tid] = rbf(s); }
    return;
  }
  b -= PRE_MOD;
  if (b < PRE_W1) { retile(w1, FF, 20, b / 3, (b % 3) * 512, 512, 0, ws + OFF_W1F, lds); return; }
  b -= PRE_W1;
  if (b < PRE_W2) {
    if (b < 48) { retile(w2, DM, 48, b, 0, 320, 1, ws + OFF_W2F, lds); __syncthreads(); retile(w2, DM, 48, b, 320, 320, 1, ws + OFF_W2F, lds); }
    return;
  }
  unsigned* ctl = (unsigned*)(ws + OFF_CTL);
  for (int i = tid; i < CT_WORDS; i += 256) if (i != CT_PAR) ctl[i] = 0u;
  const unsigned mb = mbx_base(ws);
  if (wave == 0) {
    unsigned char* p0 = ws + mb + NSLOT * MB_SLOT * 2 + (lane & 31) * 4;
    unsigned best[2] = {0xffffffffu, 0xffffffffu};
    for (int it = 0; it < 12; ++it) {
      unsigned char* p = p0 + (it & 1) * 8192;
      unsigned long long t0, t1; const unsigned v = 0;
      asm volatile("s_memtime %0\n\ts_waitcnt lgkmcnt(0)\n\tglobal_store_dword %3, %2, off sc1\n\ts_waitcnt vmcnt(0)\n\ts_memtime %1\n\ts_waitcnt lgkmcnt(0)"
                   : "=&s"(t0), "=&s"(t1) : "v"(v), "v"(p) : "memory");
      const unsigned d = (unsigned)(t1 - t0);
      if (it & 1) best[1] = min(best[1], d); else best[0] = min(best[0], d);
    }
    const unsigned near = best[1] < best[0] ? 1u : 0u;
    if (lane == 0) ctl[CT_PAR] = xcc_id() < 4 ? near : 1u - near;
  } else {
    const u32x4 sv = {SENT, SENT, SENT, SENT};
    for (int i = tid - 64; i < NSLOT * MB_SLOT / 16; i += 192) {
      const int x = i * 16;
      u32x4 v = sv;
      if (x >= MB_MA && x < MB_END) {
        const int y = (x - MB_MA) % 10240, j = y >> 7, r = (y >> 4) & 7;
        const int d0 = 64 * (j >> 3) + 16 * ((j >> 1) & 3) + 2 * (j & 1);
#pragma unroll
        for (int k = 0; k < 4; ++k) v[k] = nosent(*(const unsigned*)(mem0 + r * DM + d0 + 4 * k));
      }
      *(u32x4*)(ws + mb + l2p(x)) = v;
      *(u32x4*)(ws + mb + l2p(x) + 8192) = v;
    }
  }
}

DEV float red32(float v) {
  v += dppf<DPP_X1>(v); v += dppf<DPP_X2>(v); v += dppf<DPP_HM>(v); v += dppf<DPP_RM>(v);
  auto r = __builtin_amdgcn_permlane16_swap(fbits(v), fbits(v), false, false);
  return bitsf(r[0]) + bitsf(r[1]);
}
DEV float max32(float v) {
  v = fmaxf(v, dppf<DPP_X1>(v)); v = fmaxf(v, dppf<DPP_X2>(v)); v = fmaxf(v, dppf<DPP_HM>(v)); v = fmaxf(v, dppf<DPP_RM>(v));
  auto r = __builtin_amdgcn_permlane16_swap(fbits(v), fbits(v), false, false);
  return fmaxf(bitsf(r[0]), bitsf(r[1]));
}
DEV void ln_row(float (&v)[24], bool has3) {
  float s = 0.f;
#pragma unroll
  for (int i = 0; i < 24; ++i) s += (i < 16 || has3) ? v[i] : 0.f;
  const float mean = red32(s) * (1.f / DM);
  float q = 0.f;
#pragma unroll
  for (int i = 0; i < 24; ++i) { const float d = v[i] - mean; q += (i < 16 || has3) ? d * d : 0.f; }
  const float var = rbf(red32(q) * (1.f / DM));
  const float m = rbf(mean), den = rbf(sqrtf(rbf(var + 1e-5f)));
#pragma unroll
  for (int i = 0; i < 24; ++i) v[i] = rbf(rbf(v[i] - m) / den);
}
DEV void modulate(float (&v)[24], const float* __restrict__ sh, const float* __restrict__ scl, int l32, bool has3) {
#pragma unroll
  for (int u = 0; u < 3; ++u) {
    const int p = (u < 2 || has3) ? l32 + 32 * u : 0;
#pragma unroll
    for (int k = 0; k < 8; ++k) v[u * 8 + k] = rbf(rbf(v[u * 8 + k] * rbf(1.f + scl[p * 8 + k])) + sh[p * 8 + k]);
  }
}
DEV unsigned frag_off(int T, int p) {
  const int seg = T >> 5, tk = T & 31, tt = (tk >> 2) & 1, rho = 4 * (tk >> 3) + (tk & 3);
  return ((((unsigned)(seg * 2 + tt) * 20 + (p >> 2)) * 64) + (p & 3) * 16 + rho) * 16;
}

__global__ void __launch_bounds__(256) k_ln1(const u16* __restrict__ x, unsigned char* __restrict__ ws) {
  const int tid = threadIdx.x, l32 = tid & 31, T = blockIdx.x * 8 + (tid >> 5);
  const bool has3 = l32 < 16;
  const float* mod = (const float*)(ws + OFF_MOD);
  float v[24];
#pragma unroll
  for (int u = 0; u < 3; ++u) {
    const int p = (u < 2 || has3) ? l32 + 32 * u : 0;
    const u32x4 w = *(const u32x4*)(x + (size_t)T * DM + p * 8);
#pragma unroll
    for (int k = 0; k < 4; ++k) { v[u * 8 + 2 * k] = lo2f(w[k]); v[u * 8 + 2 * k + 1] = hi2f(w[k]); }
  }
  ln_row(v, has3);
  modulate(v, mod, mod + DM, l32, has3);
#pragma unroll
  for (int u = 0; u < 3; ++u)
    if (u < 2 || has3) {
      const u32x4 o = {pk2(v[u * 8], v[u * 8 + 1]), pk2(v[u * 8 + 2], v[u * 8 + 3]), pk2(v[u * 8 + 4], v[u * 8 + 5]), pk2(v[u * 8 + 6], v[u * 8 + 7])};
      *(u32x4*)(ws + OFF_HF + frag_off(T, l32 + 32 * u)) = o;
    }
}

__global__ void __launch_bounds__(256) k_ln2(const u16* __restrict__ x, unsigned char* __restrict__ ws) {
  const int tid = threadIdx.x, l32 = tid & 31, T = blockIdx.x * 8 + (tid >> 5);
  const bool has3 = l32 < 16;
  const float* mod = (const float*)(ws + OFF_MOD);
  float v[24];
#pragma unroll
  for (int u = 0; u < 3; ++u) {
    const int p = (u < 2 || has3) ? l32 + 32 * u : 0;
    const u32x4 w = *(const u32x4*)(x + (size_t)T * DM + p * 8);
    const unsigned char* ao = ws + OFF_AO + (((unsigned)(T >> 5) * NBC + 2 * (p >> 1)) * 32 + (T & 31)) * 16 + (p & 1) * 8;
    const u32x2 a0 = *(const u32x2*)ao, a1 = *(const u32x2*)(ao + 512);
    const unsigned a[4] = {a0.x, a1.x, a0.y, a1.y};
#pragma unroll
    for (int k = 0; k < 4; ++k) {
      v[u * 8 + 2 * k] = rbf(lo2f(w[k]) + rbf(mod[2 * DM + p * 8 + 2 * k] * lo2f(a[k])));
      v[u * 8 + 2 * k + 1] = rbf(hi2f(w[k]) + rbf(mod[2 * DM + p * 8 + 2 * k + 1] * hi2f(a[k])));
    }
    if (u < 2 || has3) {
      const u32x4 o = {pk2(v[u * 8], v[u * 8 + 1]), pk2(v[u * 8 + 2], v[u * 8 + 3]), pk2(v[u * 8 + 4], v[u * 8 + 5]), pk2(v[u * 8 + 6], v[u * 8 + 7])};
      *(u32x4*)(ws + OFF_X1 + ((size_t)T * DM + p * 8) * 2) = o;
    }
  }
  ln_row(v, has3);
  modulate(v, mod + 3 * DM, mod + 4 * DM, l32, has3);
  ln_row(v, has3);
#pragma unroll
  for (int u = 0; u < 3; ++u)
    if (u < 2 || has3) {
      const u32x4 o = {pk2(v[u * 8], v[u * 8 + 1]), pk2(v[u * 8 + 2], v[u * 8 + 3]), pk2(v[u * 8 + 4], v[u * 8 + 5]), pk2(v[u * 8 + 6], v[u * 8 + 7])};
      *(u32x4*)(ws + OFF_YF + frag_off(T, l32 + 32 * u)) = o;
    }
}

DEV float gelu_t(float x) {
  const float u = 0.7978845608028654f * (x + 0.044715f * x * x * x);
  const float e = __expf(2.f * u);
  const float th = 1.f - 2.f / (e + 1.f);
  return 0.5f * x * (1.f + th);
}
__global__ void __launch_bounds__(256) k_mlp1(const u16* __restrict__ b1, unsigned char* __restrict__ ws) {
  const int tid = threadIdx.x, lane = tid & 63, wave = tid >> 6, g = lane >> 4;
  const int tb = blockIdx.x >> 3, nb = blockIdx.x & 7;
  const int seg = tb * 2 + (wave & 1), t0 = nb * 12 + (wave >> 1) * 6;
  const u32x4* yf = (const u32x4*)(ws + OFF_YF) + (size_t)seg * 2 * 20 * 64 + lane;
  const u32x4* wf = (const u32x4*)(ws + OFF_W1F) + (size_t)t0 * 20 * 64 + lane;
  f32x4 acc[6][2];
#pragma unroll
  for (int t = 0; t < 6; ++t) { acc[t][0] = f32x4{0, 0, 0, 0}; acc[t][1] = f32x4{0, 0, 0, 0}; }
#pragma unroll 4
  for (int ks = 0; ks < 20; ++ks) {
    const u32x4 y0 = yf[ks * 64], y1 = yf[(20 + ks) * 64];
#pragma unroll
    for (int t = 0; t < 6; ++t) {
      const u32x4 w = wf[(t * 20 + ks) * 64];
      acc[t][0] = mfma32(w, y0, acc[t][0]); acc[t][1] = mfma32(w, y1, acc[t][1]);
    }
  }
#pragma unroll
  for (int tp = 0; tp < 3; ++tp) {
    float bia[8];
#pragma unroll
    for (int j = 0; j < 8; ++j) bia[j] = bf2f(b1[(t0 + 2 * tp + (j >> 2)) * 16 + 4 * g + (j & 3)]);
#pragma unroll
    for (int tt = 0; tt < 2; ++tt) {
      float o[8];
#pragma unroll
      for (int j = 0; j < 8; ++j) o[j] = gelu_t(rbf(rbf(acc[2 * tp + (j >> 2)][tt][j & 3]) + bia[j]));
      const u32x4 ov = {pk2(o[0], o[1]), pk2(o[2], o[3]), pk2(o[4], o[5]), pk2(o[6], o[7])};
      *(u32x4*)(ws + OFF_HID + ((((size_t)(seg * 2 + tt) * 48 + (t0 / 2 + tp)) * 64) + lane) * 16) = ov;
    }
  }
}
__global__ void __launch_bounds__(256) k_mlp2(const u16* __restrict__ b2, const unsigned char* __restrict__ ws, u16* __restrict__ out) {
  __shared__ __attribute__((aligned(16))) float red[4 * 20 * 64 * 4];
  const int tid = threadIdx.x, lane = tid & 63, wave = tid >> 6, g = lane >> 4, n = lane & 15;
  const int seg = blockIdx.x >> 2, nb = blockIdx.x & 3;
  const u32x4* hf = (const u32x4*)(ws + OFF_HID) + ((size_t)seg * 2 * 48 + wave * 12) * 64 + lane;
  const u32x4* wf = (const u32x4*)(ws + OFF_W2F) + ((size_t)nb * 10 * 48 + wave * 12) * 64 + lane;
  f32x4 acc[10][2];
#pragma unroll
  for (int t = 0; t < 10; ++t) { acc[t][0] = f32x4{0, 0, 0, 0}; acc[t][1] = f32x4{0, 0, 0, 0}; }
#pragma unroll 2
  for (int ks = 0; ks < 12; ++ks) {
    const u32x4 h0 = hf[ks * 64], h1 = hf[(48 + ks) * 64];
#pragma unroll
    for (int t = 0; t < 10; ++t) {
      const u32x4 w = wf[(t * 48 + ks) * 64];
      acc[t][0] = mfma32(w, h0, acc[t][0]); acc[t][1] = mfma32(w, h1, acc[t][1]);
    }
  }
#pragma unroll
  for (int t = 0; t < 10; ++t)
#pragma unroll
    for (int tt = 0; tt < 2; ++tt) *(f32x4*)&red[((wave * 20 + t * 2 + tt) * 64 + lane) * 4] = acc[t][tt];
  __syncthreads();
  const float* mod = (const float*)(ws + OFF_MOD);
#pragma unroll
  for (int i = 0; i < 5; ++i) {
    const int u = wave * 5 + i, t = u >> 1, tt = u & 1;
    f32x4 s = *(const f32x4*)&red[(u * 64 + lane) * 4];
#pragma unroll
    for (int w = 1; w < 4; ++w) s += *(const f32x4*)&red[((w * 20 + u) * 64 + lane) * 4];
    const int T = seg * 32 + tokslot(tt, n), col = nb * 160 + t * 16 + 4 * g;
    const u32x2 xv = *(const u32x2*)(ws + OFF_X1 + ((size_t)T * DM + col) * 2);
    const u32x2 bv = *(const u32x2*)(b2 + col);
    const f32x4 gb = *(const f32x4*)(mod + 5 * DM + col);
    const float o0 = lo2f(xv.x) + rbf(gb[0] * rbf(rbf(s[0]) + lo2f(bv.x))), o1 = hi2f(xv.x) + rbf(gb[1] * rbf(rbf(s[1]) + hi2f(bv.x)));
    const float o2 = lo2f(xv.y) + rbf(gb[2] * rbf(rbf(s[2]) + lo2f(bv.y))), o3 = hi2f(xv.y) + rbf(gb[3] * rbf(rbf(s[3]) + hi2f(bv.y)));
    *(u32x2*)(out + (size_t)T * DM + col) = u32x2{pk2(o0, o1), pk2(o2, o3)};
  }
}

constexpr int REC_LDS = 160 * 1024;
constexpr float SCALE = 0.07216878364870322f;
constexpr float LOG2E = 1.4426950408889634f;

struct Ctx { rsrc_t rw; LAS unsigned char* lds; unsigned ldsb; int lane, wave; unsigned mb; bool dead; };

DEV void give_up(Ctx& c, unsigned code) { c.dead = true; if (c.lane == 0) st4c(c.rw, (CT_TMO) * 4, OFF_CTL, code); }
DEV unsigned wait_prog(Ctx& c, int kind, int h0, int nhmask, unsigned need, unsigned have) {
  if (have >= need || c.dead) return have;
  const int voff = (CT_PROG + (kind * 32 + h0 + (c.lane & nhmask)) * 32) * 4;
  for (unsigned spins = 0;;) {
    unsigned v = ld4c(c.rw, voff, OFF_CTL);
    if (nhmask) { v = min(v, (unsigned)__builtin_amdgcn_update_dpp(0, (int)v, DPP_X1, 0xf, 0xf, true));
                  v = min(v, (unsigned)__builtin_amdgcn_update_dpp(0, (int)v, DPP_X2, 0xf, 0xf, true));
                  v = min(v, (unsigned)__builtin_amdgcn_update_dpp(0, (int)v, DPP_HM, 0xf, 0xf, true)); }
    v = __builtin_amdgcn_readfirstlane(v);
    if (v >= need) { asm volatile("" ::: "memory"); return v; }
    __builtin_amdgcn_s_sleep(2);
    if (++spins > SPIN_CAP) { give_up(c, 0x100u + kind); return need; }
  }
}
DEV void publish_prog(Ctx& c, int kind, int h, unsigned val) {
  waitvm<0>();
  bar();
  if (c.wave == 0 && c.lane == 0) st4c(c.rw, (CT_PROG + (kind * 32 + h) * 32) * 4, OFF_CTL, val);
}

DEV void unit48_voff(int lane, int (&voff)[3]) {
#pragma unroll
  for (int i = 0; i < 3; ++i) {
    const int p = i * 64 + lane, ir = p / 6, ch = p % 6;
    const int kr = 8 * (2 * (ir >> 4) + ((ir >> 2) & 1)) + 4 * ((ir >> 3) & 1) + (ir & 3);
    voff[i] = kr * (LDW * 2) + ch * 16;
  }
}
DEV void unit48_issue(rsrc_t rW, const int (&voff)[3], int soff, unsigned ldsaddr) {
#pragma unroll
  for (int i = 0; i < 3; ++i) dma16(rW, voff[i], soff, ldsaddr + i * 1024);
}
DEV u32x4 unit48_frag(LAS unsigned char* base, int lane, int t) {
  const int g = lane >> 4, q = (lane & 15) >> 2, pp = lane & 3;
  LAS unsigned char* a = base + (16 * (g >> 1) + 4 * (g & 1) + q) * 96 + t * 32 + pp * 8;
  return cat(trread(a), trread(a + 8 * 96));
}

constexpr bool a_inlds(int m, int sg) { return m == 0 ? (sg == 4 || sg == 6 || sg == 8) : (sg == 5 || sg == 7); }
constexpr int a_ldsidx(int m, int sg) { return sg - 4; }
constexpr int a_regidx(int m, int sg) { int n = 0; for (int mm = 0; mm < 2; ++mm) for (int s = 0; s < 10; ++s) { if (mm == m && s == sg) return n; if (!a_inlds(mm, s)) ++n; } return -1; }
constexpr int a_ord_m(int i) { int n = 0; if (i >= 15) return ((i - 15) & 1); for (int mm = 0; mm < 2; ++mm) for (int s = 0; s < 10; ++s) if (!a_inlds(mm, s)) { if (n == i) return mm; ++n; } return 0; }
constexpr int a_ord_s(int i) { int n = 0; if (i >= 15) return 4 + (i - 15); for (int mm = 0; mm < 2; ++mm) for (int s = 0; s < 10; ++s) if (!a_inlds(mm, s)) { if (n == i) return s; ++n; } return 0; }
constexpr int A_RES = 0, A_MBUF = 4 * 30720, A_SP = A_MBUF + 10240;

template <int I> struct ALoad {
  DEV static void issue(rsrc_t rq, rsrc_t rk, const int (&voff)[3], int colb, int wave, unsigned ldsw) {
    constexpr int m = a_ord_m(I), sg = a_ord_s(I);
    int t = 3 * wave + sg; t = t >= 10 ? t - 10 : t;
    const int soff = t * 64 * (LDW * 2) + colb;
    unit48_issue(m ? rk : rq, voff, soff, ldsw + (I % 5) * 6144);
    unit48_issue(m ? rk : rq, voff, soff + 32 * (LDW * 2), ldsw + (I % 5) * 6144 + 3072);
  }
};
template <int I> DEV void a_load_step(u32x8 (&W)[45], rsrc_t rq, rsrc_t rk, const int (&voff)[3], int colb, Ctx& c) {
  constexpr int left = 19 - I < 4 ? 19 - I : 4;
  waitvm<6 * left>();
  LAS unsigned char* base = c.lds + A_RES + c.wave * 30720 + (I % 5) * 6144;
  u32x8 f[3];
#pragma unroll
  for (int t = 0; t < 3; ++t) f[t] = cat8(unit48_frag(base, c.lane, t), unit48_frag(base + 3072, c.lane, t));
  if constexpr (I < 15) {
#pragma unroll
    for (int t = 0; t < 3; ++t) W[I * 3 + t] = f[t];
    asm volatile("s_waitcnt lgkmcnt(0)" : "+v"(W[I * 3]), "+v"(W[I * 3 + 1]), "+v"(W[I * 3 + 2]) :: "memory");
    ALoad<I + 5>::issue(rq, rk, voff, colb, c.wave, c.ldsb + A_RES + c.wave * 30720);
  } else {
    asm volatile("s_waitcnt lgkmcnt(0)" : "+v"(f[0]), "+v"(f[1]), "+v"(f[2]) :: "memory");
#pragma unroll
    for (int t = 0; t < 3; ++t) {
      *(LAS u32x4*)(base + t * 2048 + c.lane * 16) = u32x4{f[t][0], f[t][1], f[t][2], f[t][3]};
      *(LAS u32x4*)(base + t * 2048 + 1024 + c.lane * 16) = u32x4{f[t][4], f[t][5], f[t][6], f[t][7]};
    }
  }
  if constexpr (I < 19) a_load_step<I + 1>(W, rq, rk, voff, colb, c);
}
template <int M_, int SG, int T> DEV u32x8 a_w(const u32x8 (&W)[45], LAS unsigned char* resw) {
  if constexpr (a_inlds(M_, SG)) {
    LAS unsigned char* p = resw + a_ldsidx(M_, SG) * 6144 + T * 2048;
    return cat8(*(LAS u32x4*)p, *(LAS u32x4*)(p + 1024));
  } else return W[a_regidx(M_, SG) * 3 + T];
}
template <int P> DEV void a_pair(const u32x8 (&W)[45], LAS unsigned char* resw, const u32x4 (&mf)[10], f32x4 (&aq)[3], f32x4 (&ak)[3], int idx) {
#define A_STEP(SG) aq[P] = smfma(mf[SG], a_w<0, SG, P>(W, resw), aq[P], idx); ak[P] = smfma(mf[SG], a_w<1, SG, P>(W, resw), ak[P], idx);
  A_STEP(3) A_STEP(4) A_STEP(5) A_STEP(6) A_STEP(7) A_STEP(8) A_STEP(9)
#undef A_STEP
}

DEV void role_A(Ctx& c, int h, const u16* Wq, const u16* Wk) {
  const int lane = c.lane, wave = c.wave, g = lane >> 4, n = lane & 15;
  u32x8 W[45];
  {
    const rsrc_t rq = mkrsrc(Wq, DM * LDW * 2), rk = mkrsrc(Wk, DM * LDW * 2);
    int voff[3]; unit48_voff(lane, voff);
    const int colb = (h * DH + 48 * wave) * 2;
    const unsigned ldsw = c.ldsb + A_RES + wave * 30720;
    ALoad<0>::issue(rq, rk, voff, colb, wave, ldsw); ALoad<1>::issue(rq, rk, voff, colb, wave, ldsw); ALoad<2>::issue(rq, rk, voff, colb, wave, ldsw);
    ALoad<3>::issue(rq, rk, voff, colb, wave, ldsw); ALoad<4>::issue(rq, rk, voff, colb, wave, ldsw);
    a_load_step<0>(W, rq, rk, voff, colb, c);
  }
  LAS unsigned char* resw = c.lds + A_RES + wave * 30720 + lane * 16;
  const int idx = (lane & 8) ? 0xEEEE : 0x4444;
  const u32x2 ident = {(4 * g == n ? 0x3F80u : 0u) | (4 * g + 1 == n ? 0x3F800000u : 0u), (4 * g + 2 == n ? 0x3F80u : 0u) | (4 * g + 3 == n ? 0x3F800000u : 0u)};
  int tf[10];
#pragma unroll
  for (int sg = 0; sg < 10; ++sg) { int t = 3 * wave + sg; tf[sg] = t >= 10 ? t - 10 : t; }
  int moff[3];
#pragma unroll
  for (int sg = 0; sg < 3; ++sg) moff[sg] = l2p(MB_MA + tf[sg] * 1024 + lane * 16);
  const int qi = lane >> 5, cc = lane & 31, row = 2 * wave + qi;
  const int poff = l2p(MB_P + h * 640 + ((cc < 20 ? cc : 0) >> 2) * 128 + row * 16 + (cc & 3) * 4);
  const int roff = l2p(MB_P + h * 640 + (lane < 40 ? lane : 0) * 16);
  unsigned haveK = 0;
  for (int s = 0; s < NSEG; ++s) {
    const int slot = c.mb + (s & 3) * (MB_SLOT * 2);
    haveK = wait_prog(c, PG_K, h, 0, s + 1, haveK);
    u32x4 kx[3];
#pragma unroll
    for (int i = 0; i < 3; ++i) kx[i] = ld16c(c.rw, lane * 16 + i * 1024, OFF_KX + ((s * NH + h) * 4 + wave) * 3072);
    u32x4 mf[10];
    for (unsigned spins = 0;;) {
#pragma unroll
      for (int sg = 0; sg < 3; ++sg) mf[sg] = ld16c(c.rw, moff[sg], slot);
      const unsigned m = umax3(mx4(mf[0]), mx4(mf[1]), mx4(mf[2]));
      if (__all(m != SENT) || c.dead) break;
      if (++spins > SPIN_CAP) { give_up(c, 0x200u + s); break; }
    }
#pragma unroll
    for (int sg = 0; sg < 3; ++sg) *(LAS u32x4*)(c.lds + A_MBUF + tf[sg] * 1024 + lane * 16) = mf[sg];
    f32x4 aq[3], ak[3];
#pragma unroll
    for (int t = 0; t < 3; ++t) { aq[t] = f32x4{0, 0, 0, 0}; ak[t] = f32x4{0, 0, 0, 0}; }
#define A_K(SG) aq[0] = smfma(mf[SG], a_w<0, SG, 0>(W, resw), aq[0], idx); ak[0] = smfma(mf[SG], a_w<1, SG, 0>(W, resw), ak[0], idx); \
                aq[1] = smfma(mf[SG], a_w<0, SG, 1>(W, resw), aq[1], idx); ak[1] = smfma(mf[SG], a_w<1, SG, 1>(W, resw), ak[1], idx); \
                aq[2] = smfma(mf[SG], a_w<0, SG, 2>(W, resw), aq[2], idx); ak[2] = smfma(mf[SG], a_w<1, SG, 2>(W, resw), ak[2], idx);
    A_K(0)
    bar();
#pragma unroll
    for (int sg = 3; sg < 10; ++sg) mf[sg] = *(LAS u32x4*)(c.lds + A_MBUF + tf[sg] * 1024 + lane * 16);
    A_K(1) A_K(2)
#undef A_K
    const f32x4 z4 = {0, 0, 0, 0};
    u32x2 T[3];
    a_pair<0>(W, resw, mf, aq, ak, idx);
    a_pair<1>(W, resw, mf, aq, ak, idx);
    T[0] = pk4(mfma16(pk4(fold4(aq[0], ak[0])), ident, z4));
    a_pair<2>(W, resw, mf, aq, ak, idx);
    T[1] = pk4(mfma16(pk4(fold4(aq[1], ak[1])), ident, z4));
    const u32x4 T01 = cat(T[0], T[1]);
    f32x4 smm = mfma32(T01, T01, z4), sx0 = mfma32(kx[0], T01, z4), sx1 = mfma32(kx[1], T01, z4);
    T[2] = pk4(mfma16(pk4(fold4(aq[2], ak[2])), ident, z4));
    const u32x4 T2a = {T[2].x, T[2].y, 0u, 0u}, T2b = {0u, 0u, T[2].x, T[2].y};
    smm = mfma32(T2a, T2a, smm);
    sx0 = mfma32(kx[2], T2a, sx0);
    sx1 = mfma32(kx[2], T2b, sx1);
    if (n < 8) {
      LAS unsigned char* sp = c.lds + A_SP + (wave * 8 + n) * 160;
      *(LAS f32x4*)(sp + (8 + 8 * g) * 4) = sx0;
      *(LAS f32x4*)(sp + (8 + 8 * g + 4) * 4) = sx1;
      if (g >= 2) *(LAS f32x4*)(sp + (g - 2) * 16) = smm;
    }
    bar();
    if (s + 1 < NSEG) {
      LAS unsigned char* sp = c.lds + A_SP + row * 160 + (cc < 20 ? cc : 0) * 8;
      const f32x2 p0 = *(LAS f32x2*)sp, p1 = *(LAS f32x2*)(sp + 1280), p2 = *(LAS f32x2*)(sp + 2560), p3 = *(LAS f32x2*)(sp + 3840);
      const f32x2 sum = (p0 + p1) + (p2 + p3);
      const unsigned r1 = pk2(sum[0], sum[1]);
      const unsigned r2 = pk2(lo2f(r1) * SCALE, hi2f(r1) * SCALE);
      const float s0 = cc < 20 ? lo2f(r2) : -INFINITY, s1 = cc < 20 ? hi2f(r2) : -INFINITY;
      const float mx = max32(fmaxf(s0, s1));
      const float e0 = __builtin_amdgcn_exp2f((s0 - mx) * LOG2E), e1 = __builtin_amdgcn_exp2f((s1 - mx) * LOG2E);
      const float inv = __builtin_amdgcn_rcpf(red32(e0 + e1));
      if (cc < 20) st4c(c.rw, poff, slot, nosent(pk2(e0 * inv, e1 * inv)));
    }
    st16c(c.rw, lane * 16, OFF_KM + ((s * NH + h) * 4 + wave) * 1536, T01);
    st8c(c.rw, 1024 + lane * 8, OFF_KM + ((s * NH + h) * 4 + wave) * 1536, T[2]);
    if (wave == 1 && lane < 40) st16c(c.rw, roff, c.mb + ((s + 2) & 3) * (MB_SLOT * 2), u32x4{SENT, SENT, SENT, SENT});
    publish_prog(c, PG_KM, h, s + 1);
  }
}

constexpr int B_RES = 0, B_RING = 32768, B_MBUF = B_RING + 4 * 20480, B_RED = B_MBUF + 10240;
DEV void b_reduce_store(f32x4 d, LAS unsigned char* red, int wave, int lane) {
  const int g = lane >> 4, half = (lane >> 3) & 1, i = lane & 7;
  if ((g >> 1) == half) *(LAS f32x4*)(red + (wave * 2 + half) * 320 + (i * 2 + (g & 1)) * 16) = d;
}
DEV u32x2 b_reduce_load(LAS unsigned char* red, int lane) {
  const int o = lane >> 2, pp = lane & 3;
  f32x4 v = *(LAS f32x4*)(red + pp * 320 + o * 16) + *(LAS f32x4*)(red + (pp + 4) * 320 + o * 16);
#pragma unroll
  for (int k = 0; k < 4; ++k) { v[k] += dppf<DPP_X1>(v[k]); v[k] += dppf<DPP_X2>(v[k]); }
  return pk4(v);
}
DEV void b_outx(Ctx& c, int j, int sx, unsigned& havePX) {
  const int lane = c.lane, wave = c.wave;
  havePX = wait_prog(c, PG_PX, 8 * wave, 7, sx + 1, havePX);
  const int pv = (lane >> 3) * 640 + (lane & 7) * 16;
  u32x4 z[5];
#pragma unroll
  for (int f = 0; f < 5; ++f) z[f] = *(LAS u32x4*)(c.lds + B_RING + (sx & 3) * 20480 + (wave * 5 + f) * 1024 + lane * 16);
  f32x4 d[4];
#pragma unroll
  for (int qg = 0; qg < 4; ++qg) {
    d[qg] = f32x4{0, 0, 0, 0};
#pragma unroll
    for (int f = 0; f < 5; ++f)
      if (f <= qg + 1) d[qg] = mfma32(z[f], ld16c(c.rw, pv + f * 128, OFF_PX + ((sx * 4 + qg) * NH + 8 * wave) * 640), d[qg]);
  }
#pragma unroll
  for (int qg = 0; qg < 4; ++qg) b_reduce_store(d[qg], c.lds + B_RED + (1 + qg) * 2560, wave, lane);
  bar();
  const u32x2 o = b_reduce_load(c.lds + B_RED + (1 + wave) * 2560, lane);
  if ((lane & 3) == 0) st8(c.rw, (8 * wave) * 16 + (lane >> 2) * 8, OFF_AO + (sx * NBC + j) * 512, o);
  bar();
}

DEV void role_B(Ctx& c, int j) {
  const int lane = c.lane, wave = c.wave;
  int tf[10];
#pragma unroll
  for (int sg = 0; sg < 10; ++sg) { int t = 3 * wave + sg; tf[sg] = t >= 10 ? t - 10 : t; }
  u32x8 W[4][9];
  const int wbase = OFF_WVO + (j * 16 + wave * 4) * 20 * 1024;
#pragma unroll
  for (int tau = 0; tau < 4; ++tau) {
#pragma unroll
    for (int sg = 0; sg < 9; ++sg)
      W[tau][sg] = cat8(ld16(c.rw, lane * 16, wbase + (tau * 20 + tf[sg] * 2) * 1024), ld16(c.rw, lane * 16, wbase + (tau * 20 + tf[sg] * 2 + 1) * 1024));
    dma16(c.rw, lane * 16, wbase + (tau * 20 + tf[9] * 2) * 1024, c.ldsb + B_RES + (wave * 4 + tau) * 2048);
    dma16(c.rw, lane * 16, wbase + (tau * 20 + tf[9] * 2 + 1) * 1024, c.ldsb + B_RES + (wave * 4 + tau) * 2048 + 1024);
  }
  waitvm<0>();
  LAS unsigned char* resw = c.lds + B_RES + wave * 8192 + lane * 16;
  const int idx = (lane & 8) ? 0xEEEE : 0x4444;
  int moff[3], poff[5];
#pragma unroll
  for (int sg = 0; sg < 3; ++sg) moff[sg] = l2p(MB_MB + tf[sg] * 1024 + lane * 16);
#pragma unroll
  for (int f = 0; f < 5; ++f) poff[f] = l2p(MB_P + (8 * wave + (lane >> 3)) * 640 + f * 128 + (lane & 7) * 16);
  const int ooff = l2p(((lane & 1) ? MB_MB : MB_MA) + j * 128 + (lane >> 2) * 8);
  const int roff = l2p(((lane & 8) ? MB_MB : MB_MA) + j * 128 + (lane & 7) * 16);
  const int uv = (lane >> 3) * 512 + (lane & 7) * 16;
  unsigned haveU = 0, havePX = 0;
  for (int s = 0; s < NSEG; ++s) {
    const int slot = c.mb + (s & 3) * (MB_SLOT * 2);
    LAS unsigned char* ring = c.lds + B_RING + (s & 3) * 20480 + wave * 5120 + lane * 16;
    haveU = wait_prog(c, PG_U, 8 * wave, 7, s + 1, haveU);
    u32x4 z[5];
#pragma unroll
    for (int f = 1; f < 5; ++f) z[f] = ld16c(c.rw, uv + (f - 1) * 128, OFF_U + ((s * NBC + j) * NH + 8 * wave) * 512);
    u32x4 mf[10];
    for (unsigned spins = 0;;) {
#pragma unroll
      for (int sg = 0; sg < 3; ++sg) mf[sg] = ld16c(c.rw, moff[sg], slot);
      const unsigned m = umax3(mx4(mf[0]), mx4(mf[1]), mx4(mf[2]));
      if (__all(m != SENT) || c.dead) break;
      if (++spins > SPIN_CAP) { give_up(c, 0x300u + s); break; }
    }
#pragma unroll
    for (int sg = 0; sg < 3; ++sg) *(LAS u32x4*)(c.lds + B_MBUF + tf[sg] * 1024 + lane * 16) = mf[sg];
    bar();
#pragma unroll
    for (int sg = 3; sg < 10; ++sg) mf[sg] = *(LAS u32x4*)(c.lds + B_MBUF + tf[sg] * 1024 + lane * 16);
    f32x4 y[4];
#pragma unroll
    for (int tau = 0; tau < 4; ++tau) y[tau] = f32x4{0, 0, 0, 0};
#pragma unroll
    for (int sg = 0; sg < 9; ++sg)
#pragma unroll
      for (int tau = 0; tau < 4; ++tau) y[tau] = smfma(mf[sg], W[tau][sg], y[tau], idx);
#pragma unroll
    for (int tau = 0; tau < 4; ++tau) y[tau] = smfma(mf[9], cat8(*(LAS u32x4*)(resw + tau * 2048), *(LAS u32x4*)(resw + tau * 2048 + 1024)), y[tau], idx);
    {
      const u32x2 X = pk4(fold4(y[0], y[2])), X2 = pk4(fold4(y[1], y[3]));
      auto r0 = __builtin_amdgcn_permlane16_swap(X.x, X2.x, false, false);
      auto r1 = __builtin_amdgcn_permlane16_swap(X.y, X2.y, false, false);
      z[0] = u32x4{r0[0], r1[0], r0[1], r1[1]};
    }
#pragma unroll
    for (int f = 0; f < 5; ++f) *(LAS u32x4*)(ring + f * 1024) = z[f];
    if (s >= LAG) b_outx(c, j, s - LAG, havePX);
    if (s + 1 < NSEG) {
      u32x4 pf[5];
      for (unsigned spins = 0;;) {
#pragma unroll
        for (int f = 0; f < 5; ++f) pf[f] = ld16c(c.rw, poff[f], slot);
        const unsigned m = umax3(umax3(mx4(pf[0]), mx4(pf[1]), mx4(pf[2])), mx4(pf[3]), mx4(pf[4]));
        if (__all(m != SENT) || c.dead) break;
        if (++spins > SPIN_CAP) { give_up(c, 0x400u + s); break; }
      }
      f32x4 d = {0, 0, 0, 0};
#pragma unroll
      for (int f = 0; f < 5; ++f) d = mfma32(z[f], pf[f], d);
      b_reduce_store(d, c.lds + B_RED, wave, lane);
      bar();
      if (wave == 0) {
        u32x2 o = b_reduce_load(c.lds + B_RED, lane);
        o.x = nosent(o.x); o.y = nosent(o.y);
        if ((lane & 3) < 2) st8c(c.rw, ooff, c.mb + ((s + 1) & 3) * (MB_SLOT * 2), o);
        if (lane < 16) st16c(c.rw, roff, c.mb + ((s + 3) & 3) * (MB_SLOT * 2), u32x4{SENT, SENT, SENT, SENT});
      }
    }
  }
  waitlds();
  for (int sx = NSEG - LAG; sx < NSEG; ++sx) b_outx(c, j, sx, havePX);
}

constexpr int G_HBUF = 0, G_SP = 81920;
template <int I> DEV void g_load_step(u32x4 (&W)[20][3], rsrc_t rW, const int (&voff)[3], int colb, Ctx& c) {
  constexpr int left = 19 - I < 9 ? 19 - I : 9;
  waitvm<3 * left>();
  LAS unsigned char* base = c.lds + c.wave * 30720 + (I % 10) * 3072;
#pragma unroll
  for (int t = 0; t < 3; ++t) W[I][t] = unit48_frag(base, c.lane, t);
  asm volatile("s_waitcnt lgkmcnt(0)" : "+v"(W[I][0]), "+v"(W[I][1]), "+v"(W[I][2]) :: "memory");
  if constexpr (I + 10 < 20) unit48_issue(rW, voff, (I + 10) * 32 * (LDW * 2) + colb, c.ldsb + c.wave * 30720 + (I % 10) * 3072);
  if constexpr (I < 19) g_load_step<I + 1>(W, rW, voff, colb, c);
}
DEV void g_fetch_h(Ctx& c, int seg) {
#pragma unroll
  for (int i = 0; i < 10; ++i)
    dma16(c.rw, c.lane * 16, OFF_HF + seg * 40960 + (c.wave * 10 + i) * 1024, c.ldsb + G_HBUF + (seg & 1) * 40960 + (c.wave * 10 + i) * 1024);
}
DEV void role_G(Ctx& c, int h, int kind, const u16* Wm) {
  const int lane = c.lane, wave = c.wave, g = lane >> 4, n = lane & 15;
  u32x4 W[20][3];
  {
    const rsrc_t rW = mkrsrc(Wm, DM * LDW * 2);
    int voff[3]; unit48_voff(lane, voff);
    const int colb = (h * DH + 48 * wave) * 2;
#pragma unroll
    for (int i = 0; i < 10; ++i) unit48_issue(rW, voff, i * 32 * (LDW * 2) + colb, c.ldsb + wave * 30720 + i * 3072);
    g_load_step<0>(W, rW, voff, colb, c);
  }
  bar();
  g_fetch_h(c, 0);
  waitvm<0>();
  bar();
  unsigned haveK = 0, haveKM = 0;
  for (int s = 0; s < NSEG; ++s) {
    if (s + 1 < NSEG) g_fetch_h(c, s + 1);
    LAS unsigned char* hb = c.lds + G_HBUF + (s & 1) * 40960 + lane * 16;
    f32x4 acc[2][3];
#pragma unroll
    for (int t = 0; t < 3; ++t) { acc[0][t] = f32x4{0, 0, 0, 0}; acc[1][t] = f32x4{0, 0, 0, 0}; }
#pragma unroll
    for (int ks = 0; ks < 20; ++ks) {
      const u32x4 h0 = *(LAS u32x4*)(hb + ks * 1024), h1 = *(LAS u32x4*)(hb + (20 + ks) * 1024);
#pragma unroll
      for (int t = 0; t < 3; ++t) { acc[0][t] = mfma32(W[ks][t], h0, acc[0][t]); acc[1][t] = mfma32(W[ks][t], h1, acc[1][t]); }
    }
    u32x4 o[3];
    o[0] = cat(pk4(acc[0][0]), pk4(acc[0][1])); o[1] = cat(pk4(acc[1][0]), pk4(acc[1][1])); o[2] = cat(pk4(acc[0][2]), pk4(acc[1][2]));
    if (kind != 0) {
      const int ob = (kind == 1 ? OFF_KX : OFF_VX) + ((s * NH + h) * 4 + wave) * 3072;
#pragma unroll
      for (int i = 0; i < 3; ++i) st16c(c.rw, lane * 16 + i * 1024, ob, o[i]);
      publish_prog(c, kind == 1 ? PG_K : PG_V, h, s + 1);
    } else {
      haveK = wait_prog(c, PG_K, h, 0, s + 1, haveK);
      u32x4 kx[3];
#pragma unroll
      for (int i = 0; i < 3; ++i) kx[i] = ld16c(c.rw, lane * 16 + i * 1024, OFF_KX + ((s * NH + h) * 4 + wave) * 3072);
      const f32x4 z4 = {0, 0, 0, 0};
      f32x4 sx[2][2];
#pragma unroll
      for (int tq = 0; tq < 2; ++tq) {
        const u32x2 qb = tq ? u32x2{o[2].z, o[2].w} : u32x2{o[2].x, o[2].y};
        sx[tq][0] = mfma32(kx[2], u32x4{qb.x, qb.y, 0u, 0u}, mfma32(kx[0], o[tq], z4));
        sx[tq][1] = mfma32(kx[2], u32x4{0u, 0u, qb.x, qb.y}, mfma32(kx[1], o[tq], z4));
        LAS unsigned char* sp = c.lds + G_SP + (wave * 32 + tokslot(tq, n)) * 176;
        *(LAS f32x4*)(sp + (8 + 8 * g) * 4) = sx[tq][0];
        *(LAS f32x4*)(sp + (8 + 8 * g + 4) * 4) = sx[tq][1];
      }
      haveKM = wait_prog(c, PG_KM, h, 0, s + 1, haveKM);
      const u32x4 km = ld16c(c.rw, lane * 16, OFF_KM + ((s * NH + h) * 4 + wave) * 1536);
      const u32x2 km2 = ld8c(c.rw, 1024 + lane * 8, OFF_KM + ((s * NH + h) * 4 + wave) * 1536);
#pragma unroll
      for (int tq = 0; tq < 2; ++tq) {
        const u32x2 qb = tq ? u32x2{o[2].z, o[2].w} : u32x2{o[2].x, o[2].y};
        const f32x4 sm = mfma32(u32x4{km2.x, km2.y, 0u, 0u}, u32x4{qb.x, qb.y, 0u, 0u}, mfma32(km, o[tq], z4));
        if (g >= 2) *(LAS f32x4*)(c.lds + G_SP + (wave * 32 + tokslot(tq, n)) * 176 + (g - 2) * 16) = sm;
      }
      bar();
      {
        const int i = lane >> 3, f = lane & 7, fc = f < 5 ? f : 0, tk = 8 * wave + i;
        LAS unsigned char* sp = c.lds + G_SP + tk * 176 + fc * 32;
        f32x4 a = *(LAS f32x4*)sp, b = *(LAS f32x4*)(sp + 16);
        f32x4 a1 = *(LAS f32x4*)(sp + 5632), b1 = *(LAS f32x4*)(sp + 5632 + 16);
        f32x4 a2 = *(LAS f32x4*)(sp + 11264), b2 = *(LAS f32x4*)(sp + 11264 + 16);
        f32x4 a3 = *(LAS f32x4*)(sp + 16896), b3 = *(LAS f32x4*)(sp + 16896 + 16);
        a = (a + a1) + (a2 + a3); b = (b + b1) + (b2 + b3);
        float v[8], mx = -INFINITY;
#pragma unroll
        for (int k = 0; k < 8; ++k) {
          const float sv = rbf(rbf(k < 4 ? a[k & 3] : b[k & 3]) * SCALE);
          const bool ok = f == 0 || (f < 5 && 8 * (f - 1) + k <= tk);
          v[k] = ok ? sv : -INFINITY; mx = fmaxf(mx, v[k]);
        }
        mx = fmaxf(mx, dppf<DPP_X1>(mx)); mx = fmaxf(mx, dppf<DPP_X2>(mx)); mx = fmaxf(mx, dppf<DPP_HM>(mx));
        float sum = 0.f;
#pragma unroll
        for (int k = 0; k < 8; ++k) { v[k] = __builtin_amdgcn_exp2f((v[k] - mx) * LOG2E); sum += v[k]; }
        sum += dppf<DPP_X1>(sum); sum += dppf<DPP_X2>(sum); sum += dppf<DPP_HM>(sum);
        const float inv = __builtin_amdgcn_rcpf(sum);
        const u32x4 pv = {pk2(v[0] * inv, v[1] * inv), pk2(v[2] * inv, v[3] * inv), pk2(v[4] * inv, v[5] * inv), pk2(v[6] * inv, v[7] * inv)};
        if (f <= wave + 1) st16c(c.rw, f * 128 + i * 16, OFF_PX + ((s * 4 + wave) * NH + h) * 640, pv);
      }
      publish_prog(c, PG_PX, h, s + 1);
    }
  }
}

DEV void role_O(Ctx& c, int h, const u16* Wo) {
  const int lane = c.lane, wave = c.wave, g = lane >> 4, n = lane & 15;
  u32x4 W[6][10];
  {
    const rsrc_t rW = mkrsrc(Wo, LDW * DM * 2);
    int vrow[10], vch[10];
#pragma unroll
    for (int i = 0; i < 10; ++i) { const int p = i * 64 + lane; vrow[i] = p / 20; vch[i] = (p % 20) * 16; }
    const int colb = h * DH * DM * 2 + wave * 320;
    const unsigned ldsw = c.ldsb + wave * 30720;
#pragma unroll
    for (int half = 0; half < 2; ++half) {
#pragma unroll
      for (int k3 = 0; k3 < 3; ++k3) {
        const int ks = half * 3 + k3;
#pragma unroll
        for (int i = 0; i < 10; ++i) {
          const int e = ks < 4 ? 48 * ks + vrow[i] : 48 * (2 * (ks - 4) + (vrow[i] >> 4)) + 32 + (vrow[i] & 15);
          dma16(rW, e * (DM * 2) + vch[i], colb, ldsw + k3 * 10240 + i * 1024);
        }
      }
      waitvm<0>();
#pragma unroll
      for (int k3 = 0; k3 < 3; ++k3)
#pragma unroll
        for (int t = 0; t < 10; ++t) {
          LAS unsigned char* a = c.lds + wave * 30720 + k3 * 10240 + (4 * g + (n >> 2)) * 320 + t * 32 + (lane & 3) * 8;
          W[half * 3 + k3][t] = cat(trread(a), trread(a + 16 * 320));
        }
#pragma unroll
      for (int k3 = 0; k3 < 3; ++k3)
#pragma unroll
        for (int t = 0; t < 10; t += 2) asm volatile("s_waitcnt lgkmcnt(0)" : "+v"(W[half * 3 + k3][t]), "+v"(W[half * 3 + k3][t + 1]) :: "memory");
    }
  }
  const int ov = ((n >> 1) & 1) * (NH * 512) + g * 128 + (2 * (n >> 2) + (n & 1)) * 16;
  unsigned haveV = 0;
  for (int s = 0; s < NSEG; ++s) {
    haveV = wait_prog(c, PG_V, h, 0, s + 1, haveV);
    u32x4 vf[12];
#pragma unroll
    for (int i = 0; i < 12; ++i) vf[i] = ld16c(c.rw, lane * 16 + (i & 3) * 1024, OFF_VX + (s * NH + h) * 12288 + (i >> 2) * 4096);
    u32x4 a[2][6];
#pragma unroll
    for (int w = 0; w < 4; ++w) { a[0][w] = vf[w * 3]; a[1][w] = vf[w * 3 + 1]; }
#pragma unroll
    for (int k = 0; k < 2; ++k) {
      a[0][4 + k] = u32x4{vf[6 * k + 2].x, vf[6 * k + 2].y, vf[6 * k + 5].x, vf[6 * k + 5].y};
      a[1][4 + k] = u32x4{vf[6 * k + 2].z, vf[6 * k + 2].w, vf[6 * k + 5].z, vf[6 * k + 5].w};
    }
#pragma unroll
    for (int t = 0; t < 10; ++t) {
      f32x4 a0 = {0, 0, 0, 0}, a1 = {0, 0, 0, 0};
#pragma unroll
      for (int ks = 0; ks < 6; ++ks) { a0 = mfma32(a[0][ks], W[ks][t], a0); a1 = mfma32(a[1][ks], W[ks][t], a1); }
      st16c(c.rw, ov, OFF_U + ((s * NBC + 2 * (wave * 10 + t)) * NH + h) * 512, cat(pk4(a0), pk4(a1)));
    }
    publish_prog(c, PG_U, h, s + 1);
  }
}

__global__ void __launch_bounds__(256, 1) k_rec(const u16* __restrict__ Wq, const u16* __restrict__ Wk, const u16* __restrict__ Wv,
                                                const u16* __restrict__ Wo, unsigned char* __restrict__ ws) {
  extern __shared__ __attribute__((aligned(16))) unsigned char dyn[];
  Ctx c;
  c.lds = (LAS unsigned char*)dyn; c.ldsb = (unsigned)(size_t)c.lds;
  c.lane = threadIdx.x & 63; c.wave = __builtin_amdgcn_readfirstlane(threadIdx.x >> 6);
  c.rw = mkrsrc(ws, WS_USED); c.dead = false;
  GAS unsigned* ctl = (GAS unsigned*)(ws + OFF_CTL);
  if (threadIdx.x == 0) {
    const unsigned x = xcc_id();
    const unsigned t = __hip_atomic_fetch_add(ctl + CT_TICKET + 64 * x, 1u, RLX_AGENT);
    int role = -1;
    if (t < (x < 4 ? 28u : 32u)) role = x * 32 + t;
    else {
      unsigned k = __hip_atomic_fetch_add(ctl + CT_SPARE, 1u, RLX_AGENT);
      unsigned cnt[8];
      for (unsigned spins = 0; spins < SPIN_CAP; ++spins) {
        unsigned tot = 0;
#pragma unroll
        for (int i = 0; i < 8; ++i) { cnt[i] = __hip_atomic_load(ctl + CT_TICKET + 64 * i, RLX_AGENT); tot += cnt[i]; }
        if (tot >= gridDim.x) break;
        __builtin_amdgcn_s_sleep(4);
      }
#pragma unroll
      for (int i = 0; i < 8; ++i) {
        const unsigned cap = i < 4 ? 28u : 32u, miss = cnt[i] < cap ? cap - cnt[i] : 0u;
        if (role < 0 && k < miss) role = i * 32 + cnt[i] + k;
        if (role < 0) k -= miss;
      }
    }
    *(LAS int*)c.lds = role;
    *(LAS unsigned*)(c.lds + 4) = __hip_atomic_load(ctl + CT_PAR, RLX_AGENT);
  }
  __syncthreads();
  const int role = __builtin_amdgcn_readfirstlane(*(LAS int*)c.lds);
  c.mb = mbx_base(ws) + (__builtin_amdgcn_readfirstlane(*(LAS unsigned*)(c.lds + 4)) & 1u) * 8192;
  __syncthreads();
  if (role < 0) return;
  const int x = role >> 5, t = role & 31;
  if (x < 4) {
    if (t < 8) role_A(c, 8 * x + t, Wq, Wk);
    else role_B(c, 20 * x + t - 8);
  } else {
    const int h = 8 * (x - 4) + (t >> 2), kind = t & 3;
    if (kind == 3) role_O(c, h, Wo);
    else role_G(c, h, kind, kind == 0 ? Wq : (kind == 1 ? Wk : Wv));
  }
}

}

extern "C" void kernel_launch(void* const* d_in, const int* in_sizes, int n_in, void* d_out, int out_size, void* d_ws, size_t ws_size,
                              hipStream_t stream) {
  const u16* x = (const u16*)d_in[0];
  const u16* mem0 = (const u16*)d_in[1];
  const u16* Wq = (const u16*)d_in[2];
  const u16* Wk = (const u16*)d_in[3];
  const u16* Wv = (const u16*)d_in[4];
  const u16* Wo = (const u16*)d_in[5];
  const u16* w1 = (const u16*)d_in[8];
  const u16* b1 = (const u16*)d_in[9];
  const u16* w2 = (const u16*)d_in[10];
  const u16* b2 = (const u16*)d_in[11];
  const u16* cv = (const u16*)d_in[12];
  const u16* wmod = (const u16*)d_in[13];
  unsigned char* ws = (unsigned char*)d_ws;
  (void)hipFuncSetAttribute((const void*)k_rec, hipFuncAttributeMaxDynamicSharedMemorySize, REC_LDS);
  k_pre<<<PRE_GRID, 256, 0, stream>>>(mem0, Wv, Wo, w1, w2, cv, wmod, ws);
  k_ln1<<<NTOK / 8, 256, 0, stream>>>(x, ws);
  k_rec<<<256, 256, REC_LDS, stream>>>(Wq, Wk, Wv, Wo, ws);
  k_ln2<<<NTOK / 8, 256, 0, stream>>>(x, ws);
  k_mlp1<<<256, 256, 0, stream>>>(b1, ws);
  k_mlp2<<<256, 256, 0, stream>>>(b2, ws, (u16*)d_out);
}
```

```cpp
#include <hip/hip_runtime.h>
#include <hip/hip_bf16.h>
#include <stdint.h>

#define EXP1 0
#define EXP2 0
#define PFA 3
#define UR 7
#define UP 7
#define FLG 2
#define WFINE 1
#define WOFF (WFINE ? 3 : 0)
#define AINC 2
#define ADEC 6
#define THR 3
#define NTM 3
namespace {

typedef __attribute__((ext_vector_type(2))) __bf16 bf16x2;
typedef __attribute__((ext_vector_type(4))) __bf16 bf16x4;
typedef __attribute__((ext_vector_type(8))) __bf16 bf16x8;
typedef __attribute__((ext_vector_type(16))) __bf16 bf16x16;
typedef __attribute__((ext_vector_type(4))) short s16x4;
typedef __attribute__((ext_vector_type(2))) float f32x2;
typedef __attribute__((ext_vector_type(4))) float f32x4;
typedef __attribute__((ext_vector_type(2))) unsigned u32x2;
typedef __attribute__((ext_vector_type(4))) unsigned u32x4;
typedef __attribute__((ext_vector_type(6))) unsigned u32x6;
typedef __attribute__((ext_vector_type(8))) unsigned u32x8;
typedef unsigned short u16;
typedef __amdgpu_buffer_rsrc_t rsrc_t;
#define GAS __attribute__((address_space(1)))
#define LAS __attribute__((address_space(3)))
#define DEV __device__ __forceinline__
#define RLX_AGENT __ATOMIC_RELAXED, __HIP_MEMORY_SCOPE_AGENT

constexpr int NTOK = 2048, DM = 640, NH = 32, DH = 192, NSEG = 64, FF = 1536, NBC = 80;
constexpr int LDW = NH * DH;

constexpr unsigned OFF_CTL = 0;
constexpr unsigned OFF_MOD = 65536;
constexpr unsigned OFF_MBX = 1u << 20;
constexpr unsigned OFF_HF  = 5u << 20;
constexpr unsigned OFF_X1  = 11u << 20;
constexpr unsigned OFF_YF  = 14u << 20;
constexpr unsigned OFF_HID = 17u << 20;
constexpr unsigned OFF_WVO = 47u << 20;
constexpr unsigned OFF_KX  = 72u << 20;
constexpr unsigned OFF_VX  = 96u << 20;
constexpr unsigned OFF_U   = 120u << 20;
constexpr unsigned OFF_QX  = 200u << 20;
constexpr unsigned OFF_DBG = 224u << 20;
constexpr unsigned WS_USED = 232u << 20;

constexpr int CT_TICKET = 0;
constexpr int CT_SPARE = 512, CT_MOD = 576, CT_MOD2 = 608, CT_TMO = 640, CT_LN = 704, CT_WVO = 768, CT_PAR = 960;
enum { PG_Q = 0, PG_K = 1, PG_V = 2, PG_U = 3, PG_N = 4, PG_M1 = 5, PG_PX = 6, PG_KM = 8, PG_AO = 10 };
constexpr int CT_WORDS = 7 * 1024;

constexpr int NSLOT = 8;
constexpr int MB_MA = 21504, MB_HA = 31744, MB_HB = 31872,
              MB_P2 = 32768, MB_M2 = MB_P2 + MB_MA, MB_MB = MB_MA + 49152, MB_M3 = MB_MA + 65536;
constexpr int MB_2ND = 2 * MB_P2, MB_3RD = 2 * 65536;
constexpr int MB_SLOT = 98304;
constexpr int MB_RPT = NSLOT * MB_SLOT + 8192;
constexpr int MB_FLG = MB_RPT + 16384;
constexpr unsigned XB_DIST = 136u << 20;
constexpr int XB_AO = 196608, XB_KM = 278528;
constexpr unsigned SENT = 0xFFFFFFFFu;
constexpr unsigned SPIN_CAP = 200000u;
#define LAGV 4
constexpr int LAG = LAGV;
constexpr int RM = 7;
constexpr int OOB = 0x7FFF0000;

#define SB0() __builtin_amdgcn_sched_barrier(0)
#define MEMF() asm volatile("" ::: "memory")
template <class T> DEV void pin_a(T& v) { asm volatile("" : "+a"(v)); }
template <class T> DEV void pin_s(T& v) { asm volatile("" : "+s"(v)); }
template <class T> DEV void pin_v(T& v) { asm volatile("" : "+v"(v)); }
DEV int l2p(int x) { return ((x >> 13) << 14) + (x & 8191); }
DEV int p_head(int h) { return (h / 12) * 16384 + (h % 12) * 640; }
DEV int tokslot(int tt, int r) { return 8 * (r >> 2) + 4 * tt + (r & 3); }

DEV int krow(int k) { return (k & 16) + 8 * ((k >> 1) & 1) + 2 * ((k >> 2) & 3) + (k & 1); }
DEV float bf2f(u16 v) { return __builtin_bit_cast(float, (unsigned)v << 16); }
DEV float lo2f(unsigned v) { return __builtin_bit_cast(float, v << 16); }
DEV float hi2f(unsigned v) { return __builtin_bit_cast(float, v & 0xffff0000u); }
DEV unsigned pk2(float a, float b) { f32x2 x = {a, b}; return __builtin_bit_cast(unsigned, __builtin_convertvector(x, bf16x2)); }
DEV float rbf(float a) { return lo2f(pk2(a, a)); }
DEV unsigned nosent(unsigned v) { return min(v, 0xFFFFFFFEu); }

DEV u32x4 ld16(rsrc_t r, int v, int s) { return __builtin_amdgcn_raw_buffer_load_b128(r, v, s, 0); }
DEV u32x4 ld16n(rsrc_t r, int v, int s) { return __builtin_amdgcn_raw_buffer_load_b128(r, v, s, 2); }
DEV u32x4 ld16c(rsrc_t r, int v, int s) { return __builtin_amdgcn_raw_buffer_load_b128(r, v, s, 16); }
DEV u32x2 ld8c(rsrc_t r, int v, int s) { return __builtin_amdgcn_raw_buffer_load_b64(r, v, s, 16); }
DEV unsigned ld4c(rsrc_t r, int v, int s) { return __builtin_amdgcn_raw_buffer_load_b32(r, v, s, 16); }
DEV void st16(rsrc_t r, int v, int s, u32x4 x) { __builtin_amdgcn_raw_buffer_store_b128(x, r, v, s, 0); }
DEV void st8(rsrc_t r, int v, int s, u32x2 x) { __builtin_amdgcn_raw_buffer_store_b64(x, r, v, s, 0); }
DEV void st16c(rsrc_t r, int v, int s, u32x4 x) { __builtin_amdgcn_raw_buffer_store_b128(x, r, v, s, 16); }
DEV void st8c(rsrc_t r, int v, int s, u32x2 x) { __builtin_amdgcn_raw_buffer_store_b64(x, r, v, s, 16); }
DEV void st4c(rsrc_t r, int v, int s, unsigned x) { __builtin_amdgcn_raw_buffer_store_b32(x, r, v, s, 16); }
DEV rsrc_t mkrsrc(const void* p, unsigned bytes) { return __builtin_amdgcn_make_buffer_rsrc((void*)p, 0, (int)bytes, 0x00020000); }
DEV void dma_m0(unsigned ldsaddr) { asm volatile("s_mov_b32 m0, %0" :: "s"(ldsaddr) : "memory"); }
template <int OFF, bool NT = false> DEV void dma_off(rsrc_t r, int voff, int soff) {
  if constexpr (NT) asm volatile("buffer_load_dwordx4 %0, %1, %2 offen offset:%3 nt lds" :: "v"(voff), "s"(r), "s"(soff), "i"(OFF) : "memory");
  else asm volatile("buffer_load_dwordx4 %0, %1, %2 offen offset:%3 lds" :: "v"(voff), "s"(r), "s"(soff), "i"(OFF) : "memory"); }
template <int N, bool NT = true> DEV void dma_grp(rsrc_t r, const int (&v)[N], int soff, unsigned lds) {
#pragma unroll
  for (int i = 0; i < N; ++i) {
    if ((i & 3) == 0) asm volatile("s_mov_b32 m0, %0\n\ts_nop 4" :: "s"(lds + i * 1024), "s"(r), "s"(soff) : "memory");
    if ((i & 3) == 0) dma_off<0, NT>(r, v[i], soff);
    if ((i & 3) == 1) dma_off<1024, NT>(r, v[i], soff);
    if ((i & 3) == 2) dma_off<2048, NT>(r, v[i], soff);
    if ((i & 3) == 3) dma_off<3072, NT>(r, v[i], soff);
  }
}
template <int N> DEV void waitvm() { asm volatile("s_waitcnt vmcnt(%0)" :: "i"(N) : "memory"); }
DEV void waitlds() { asm volatile("s_waitcnt lgkmcnt(0)" ::: "memory"); }
DEV void bar_vm() { asm volatile("s_waitcnt vmcnt(0) lgkmcnt(0)\n\ts_barrier" ::: "memory"); }
DEV void bar() { asm volatile("s_waitcnt lgkmcnt(0)\n\ts_barrier" ::: "memory"); }

template <int V> DEV u32x2 imm2() { u32x2 r; asm volatile("v_mov_b64 %0, %1" : "=v"(r) : "i"(V)); return r; }
DEV unsigned umax3(unsigned a, unsigned b, unsigned c) { return max(max(a, b), c); }
DEV unsigned mx4(u32x4 v) { return max(umax3(v.x, v.y, v.z), v.w); }
DEV f32x4 mfma32(u32x4 a, u32x4 b, f32x4 c) {
  return __builtin_amdgcn_mfma_f32_16x16x32_bf16(__builtin_bit_cast(bf16x8, a), __builtin_bit_cast(bf16x8, b), c, 0, 0, 0); }
DEV f32x4 mfma16(u32x2 a, u32x2 b, f32x4 c) {
  return __builtin_amdgcn_mfma_f32_16x16x16bf16_1k(__builtin_bit_cast(s16x4, a), __builtin_bit_cast(s16x4, b), c, 0, 0, 0); }
DEV f32x4 mfma32v(u32x4 a, u32x4 b) {
  f32x4 d; asm volatile("v_mfma_f32_16x16x32_bf16 %0, %1, %2, 0" : "=&v"(d) : "v"(a), "v"(b)); return d; }
DEV f32x4 smfma(u32x4 a, u32x8 b, f32x4 c, int idx) {
  return __builtin_amdgcn_smfmac_f32_16x16x64_bf16(__builtin_bit_cast(bf16x8, a), __builtin_bit_cast(bf16x16, b), c, idx, 0, 0); }
DEV u32x2 trread(LAS unsigned char* p) { return __builtin_bit_cast(u32x2, __builtin_amdgcn_ds_read_tr16_b64_v4bf16((LAS bf16x4*)p)); }
DEV u32x4 cat(u32x2 a, u32x2 b) { return u32x4{a.x, a.y, b.x, b.y}; }
DEV u32x8 cat8(u32x4 a, u32x4 b) { return u32x8{a.x, a.y, a.z, a.w, b.x, b.y, b.z, b.w}; }
DEV u32x2 pk4(f32x4 v) { return u32x2{pk2(v[0], v[1]), pk2(v[2], v[3])}; }
template <int C> DEV float dppf(float v) {
  return __builtin_bit_cast(float, __builtin_amdgcn_update_dpp(0, __builtin_bit_cast(int, v), C, 0xf, 0xf, true)); }
constexpr int DPP_X1 = 0xB1, DPP_X2 = 0x4E, DPP_HM = 0x141, DPP_RM = 0x140;
DEV unsigned fbits(float v) { return __builtin_bit_cast(unsigned, v); }
DEV float bitsf(unsigned v) { return __builtin_bit_cast(float, v); }
DEV float fold32(float a, float b) {
  auto r = __builtin_amdgcn_permlane32_swap(fbits(a), fbits(b), false, false);
  return bitsf(r[0]) + bitsf(r[1]); }
DEV f32x4 fold4(f32x4 a, f32x4 b) { return f32x4{fold32(a[0], b[0]), fold32(a[1], b[1]), fold32(a[2], b[2]), fold32(a[3], b[3])}; }
DEV unsigned xcc_id() { return (unsigned)__builtin_amdgcn_s_getreg(20 | (3 << 11)) & 7u; }
DEV unsigned mbx_base(const unsigned char* ws) {
  const uint64_t a = (uint64_t)ws;
  return (unsigned)(((a + OFF_MBX + 0x1FFFFFull) & ~0x1FFFFFull) - a); }

__global__ void __launch_bounds__(256) k_init(unsigned char* __restrict__ ws) {
  *(u32x4*)(ws + OFF_CTL + (blockIdx.x * 256 + threadIdx.x) * 16) = u32x4{0u, 0u, 0u, 0u};
}

DEV float red32(float v) {
  v += dppf<DPP_X1>(v); v += dppf<DPP_X2>(v); v += dppf<DPP_HM>(v); v += dppf<DPP_RM>(v);
  auto r = __builtin_amdgcn_permlane16_swap(fbits(v), fbits(v), false, false);
  return bitsf(r[0]) + bitsf(r[1]);
}
DEV float max32a(float a, float b) {
  float v, t;
  asm volatile("v_max_f32 %0, %2, %3\n\ts_nop 1\n\t"
               "v_max_f32_dpp %0, %0, %0 quad_perm:[1,0,3,2] row_mask:0xf bank_mask:0xf\n\ts_nop 1\n\t"
               "v_max_f32_dpp %0, %0, %0 quad_perm:[2,3,0,1] row_mask:0xf bank_mask:0xf\n\ts_nop 1\n\t"
               "v_max_f32_dpp %0, %0, %0 row_half_mirror row_mask:0xf bank_mask:0xf\n\ts_nop 1\n\t"
               "v_max_f32_dpp %1, %0, %0 row_mirror row_mask:0xf bank_mask:0xf\n\t"
               "v_max_f32_dpp %0, %0, %0 row_mirror row_mask:0xf bank_mask:0xf\n\ts_nop 1\n\t"
               "v_permlane16_swap_b32 %0, %1\n\ts_nop 0\n\t"
               "v_max_f32 %0, %0, %1" : "=&v"(v), "=&v"(t) : "v"(a), "v"(b));
  return v;
}
DEV float sum32a(float a, float b) {
  float v, t;
  asm volatile("v_add_f32 %0, %2, %3\n\ts_nop 1\n\t"
               "v_add_f32_dpp %0, %0, %0 quad_perm:[1,0,3,2] row_mask:0xf bank_mask:0xf\n\ts_nop 1\n\t"
               "v_add_f32_dpp %0, %0, %0 quad_perm:[2,3,0,1] row_mask:0xf bank_mask:0xf\n\ts_nop 1\n\t"
               "v_add_f32_dpp %0, %0, %0 row_half_mirror row_mask:0xf bank_mask:0xf\n\ts_nop 1\n\t"
               "v_add_f32_dpp %1, %0, %0 row_mirror row_mask:0xf bank_mask:0xf\n\t"
               "v_add_f32_dpp %0, %0, %0 row_mirror row_mask:0xf bank_mask:0xf\n\ts_nop 1\n\t"
               "v_permlane16_swap_b32 %0, %1\n\ts_nop 0\n\t"
               "v_add_f32 %0, %0, %1" : "=&v"(v), "=&v"(t) : "v"(a), "v"(b));
  return v;
}
DEV void rbf2(float& a, float& b) { const unsigned r = pk2(a, b); a = lo2f(r); b = hi2f(r); }
DEV void ln_row(float (&v)[24], bool has3) {
  float s0 = 0.f, s1 = 0.f;
#pragma unroll
  for (int i = 0; i < 16; ++i) s0 += v[i];
#pragma unroll
  for (int i = 16; i < 24; ++i) s1 += v[i];
  const float mean = red32(s0 + (has3 ? s1 : 0.f)) * (1.f / DM);
  float q0 = 0.f, q1 = 0.f;
#pragma unroll
  for (int i = 0; i < 16; ++i) { const float d = v[i] - mean; q0 = __builtin_fmaf(d, d, q0); }
#pragma unroll
  for (int i = 16; i < 24; ++i) { const float d = v[i] - mean; q1 = __builtin_fmaf(d, d, q1); }
  const float var = rbf(red32(q0 + (has3 ? q1 : 0.f)) * (1.f / DM));
  const float m = rbf(mean), inv = 1.f / rbf(sqrtf(rbf(var + 1e-5f)));
#pragma unroll
  for (int i = 0; i < 24; i += 2) {
    float a = v[i] - m, b = v[i + 1] - m; rbf2(a, b);
    a *= inv; b *= inv; rbf2(a, b);
    v[i] = a; v[i + 1] = b;
  }
}
DEV void modulate(float (&v)[24], const float (&sh)[24], const float (&scl)[24]) {
#pragma unroll
  for (int i = 0; i < 24; i += 2) {
    float c0 = 1.f + scl[i], c1 = 1.f + scl[i + 1]; rbf2(c0, c1);
    float a = v[i] * c0, b = v[i + 1] * c1; rbf2(a, b);
    a += sh[i]; b += sh[i + 1]; rbf2(a, b);
    v[i] = a; v[i + 1] = b;
  }
}
DEV unsigned frag_off(int T, int p) {
  const int seg = T >> 5, tk = T & 31, tt = (tk >> 2) & 1, rho = 4 * (tk >> 3) + (tk & 3);
  return ((((unsigned)(seg * 2 + tt) * 20 + (p >> 2)) * 64) + (p & 3) * 16 + rho) * 16;
}

DEV u32x4 pk8(const float* v) { return u32x4{pk2(v[0], v[1]), pk2(v[2], v[3]), pk2(v[4], v[5]), pk2(v[6], v[7])}; }
DEV float gelu_t(float x) {
  const float t = x * __builtin_fmaf(x * x, -0.044715f * 2.302208198f, -2.302208198f);
  return x * __builtin_amdgcn_rcpf(1.f + __builtin_amdgcn_exp2f(t));
}

#define UDLY 4
#define PLACE 1
constexpr int REC_LDS = 160 * 1024;
constexpr float SCALE = 0.07216878364870322f;
constexpr float LOG2E = 1.4426950408889634f;
template <bool V> struct BoolC { static constexpr bool v = V; };
#define SGB(m, n) __builtin_amdgcn_sched_group_barrier(m, n, 0)

struct Ctx { rsrc_t rw; LAS unsigned char* lds; unsigned ldsb; int lane, wave, role, flip; unsigned mb; bool dead; int pLA, pLB, pDM, pPen, pMA, pMB, pInc, pD; };

#define MS(k_) do {} while (0)
DEV unsigned rtnow() { return (unsigned)__builtin_amdgcn_s_memrealtime() & 0x7fffffffu; }
DEV int rtdiff(unsigned a, unsigned b) { return (int)((a - b) << 1) >> 1; }
DEV void wait_raw(unsigned T) {
  unsigned d;
  asm volatile("1:\n\ts_memrealtime vcc\n\ts_waitcnt lgkmcnt(0)\n\t"
               "s_sub_u32 %0, %1, vcc_lo\n\ts_bitset0_b32 %0, 31\n\ts_cmp_lt_u32 %0, 2000\n\ts_cbranch_scc1 1b\n\t"
               "s_add_u32 %0, %0, 3\n\ts_bitset0_b32 %0, 31\n\t"
               "s_cmp_gt_u32 %0, 2\n\ts_cbranch_scc1 2f\n\ts_nop 2\n\t"
               "s_cmp_eq_u32 %0, 0\n\ts_cbranch_scc1 2f\n\ts_nop 2\n\t"
               "s_cmp_eq_u32 %0, 1\n\ts_cbranch_scc1 2f\n\ts_nop 8\n2:"
               : "=&s"(d) : "s"(T - 5u) : "scc", "vcc", "memory");
}
constexpr int WF_MIN = 13;
DEV void wait_fine(unsigned T, int d0) {
  unsigned long long a0, a1, a2, a3, a4, a5, a6;
  unsigned d;
  if (d0 > 24)
    asm volatile("1:\n\ts_memrealtime vcc\n\ts_waitcnt lgkmcnt(0)\n\t"
                 "s_sub_u32 %0, %1, vcc_lo\n\ts_bitset0_b32 %0, 31\n\ts_cmp_lt_u32 %0, 2000\n\ts_cbranch_scc1 1b"
                 : "=&s"(d) : "s"(T - 22u) : "scc", "vcc", "memory");
  asm volatile("s_memrealtime %0\n\ts_nop 2\n\ts_memrealtime %1\n\ts_nop 2\n\ts_memrealtime %2\n\ts_nop 2\n\ts_memrealtime %3\n\ts_nop 2\n\t"
               "s_memrealtime %4\n\ts_nop 2\n\ts_memrealtime %5\n\ts_nop 2\n\ts_memrealtime %6\n\ts_waitcnt lgkmcnt(0)"
               : "=&s"(a0), "=&s"(a1), "=&s"(a2), "=&s"(a3), "=&s"(a4), "=&s"(a5), "=&s"(a6) :: "memory");
  const unsigned v = (unsigned)a6;
  unsigned n;
  asm volatile("s_cmp_eq_u32 %2, %1\n\ts_cselect_b32 %0, 1, 0\n\ts_cmp_eq_u32 %3, %1\n\ts_addc_u32 %0, %0, 0\n\ts_cmp_eq_u32 %4, %1\n\ts_addc_u32 %0, %0, 0\n\t"
               "s_cmp_eq_u32 %5, %1\n\ts_addc_u32 %0, %0, 0\n\ts_cmp_eq_u32 %6, %1\n\ts_addc_u32 %0, %0, 0\n\ts_cmp_eq_u32 %7, %1\n\ts_addc_u32 %0, %0, 0"
               : "=&s"(n) : "s"(v), "s"((unsigned)a0), "s"((unsigned)a1), "s"((unsigned)a2), "s"((unsigned)a3), "s"((unsigned)a4), "s"((unsigned)a5) : "scc");
  unsigned k;
  asm volatile("s_sub_u32 %0, %1, %2\n\ts_and_b32 %0, %0, 31\n\ts_mulk_i32 %0, 88\n\ts_lshr_b32 %0, %0, 5\n\t"
               "s_lshl_b32 vcc_lo, %3, 1\n\ts_add_u32 vcc_lo, vcc_lo, 60\n\ts_sub_i32 %0, vcc_lo, %0\n\t"
               "s_max_i32 %0, %0, 0\n\ts_min_i32 %0, %0, 47\n\ts_lshl_b32 %0, %0, 2\n\t"
               "s_getpc_b64 vcc\n\ts_add_u32 %0, %0, 16\n\ts_add_u32 vcc_lo, vcc_lo, %0\n\ts_addc_u32 vcc_hi, vcc_hi, 0\n\ts_setpc_b64 vcc\n\t"
               ".rept 48\n\ts_nop 1\n\t.endr"
               : "=&s"(k) : "s"(T), "s"(v), "s"(n) : "scc", "vcc", "memory");
}
DEV bool wait_rt(unsigned T) {
  const int d0 = rtdiff(T, rtnow());
  const bool ontime = d0 > 4 && d0 < 2000;
  if (ontime) wait_raw(T);
  return ontime;
}
DEV unsigned wave_umax(unsigned v) {
  v = max(v, (unsigned)__builtin_amdgcn_update_dpp(0, (int)v, DPP_X1, 0xf, 0xf, true)); v = max(v, (unsigned)__builtin_amdgcn_update_dpp(0, (int)v, DPP_X2, 0xf, 0xf, true));
  v = max(v, (unsigned)__builtin_amdgcn_update_dpp(0, (int)v, DPP_HM, 0xf, 0xf, true)); v = max(v, (unsigned)__builtin_amdgcn_update_dpp(0, (int)v, DPP_RM, 0xf, 0xf, true));
  return max(max((unsigned)__builtin_amdgcn_readlane(v, 0), (unsigned)__builtin_amdgcn_readlane(v, 16)), max((unsigned)__builtin_amdgcn_readlane(v, 32), (unsigned)__builtin_amdgcn_readlane(v, 48)));
}
DEV unsigned cyc() { return (unsigned)__builtin_amdgcn_s_memtime(); }
struct Pace { unsigned wprev, sprev; float r; };
DEV unsigned pace_report(Pace& p, int s, unsigned sched, unsigned wake, unsigned done, bool bad, int d0) {
  const int extra = max(4 - d0, 0);
  bad = bad || extra > 24 || d0 >= 2000;
  const bool clean = !bad && d0 > 4;
  if (clean && p.sprev != SENT) p.r = (float)rtdiff(sched, p.sprev) * __builtin_amdgcn_rcpf((float)(wake - p.wprev));
  const unsigned lat = min((unsigned)((float)(done - wake) * p.r) + 1u + (unsigned)extra, 0x3fffu);
  p.wprev = wake; p.sprev = clean ? sched : SENT;
  return ((unsigned)(s + 1) << 16) | lat | (bad ? 0x4000u : 0u);
}
DEV void give_up(Ctx& c, unsigned code) { c.dead = true; st4c(c.rw, (CT_TMO) * 4, OFF_CTL, code); }
DEV bool all_valid(unsigned m) { return __builtin_amdgcn_ballot_w64(m == SENT) == 0ull; }
DEV int pgoff(const Ctx& c, int kind, int h) { return kind < PG_PX ? 4096 + kind * 4096 + h * 128 : (int)c.mb + l2p(MB_FLG + (kind - PG_PX) * 4096 + h * (kind == PG_AO ? 64 : 128)); }
DEV int xblk(const Ctx& c, int s) { return (int)(c.mb + XB_DIST) + s * 524288 + ((s & 4) ? c.flip : 0); }
DEV int km_piece(int q) { return XB_KM + (q / 10) * 16384 + (q % 10) * 768; }
DEV unsigned prog_issue(Ctx& c, int kind, int h0, int nhmask) { return ld4c(c.rw, pgoff(c, kind, h0 + (c.lane & nhmask)), OFF_CTL); }
DEV unsigned prog_finish(unsigned v, int nhmask) {
  if (nhmask) { v = min(v, (unsigned)__builtin_amdgcn_update_dpp(0, (int)v, DPP_X1, 0xf, 0xf, true));
                v = min(v, (unsigned)__builtin_amdgcn_update_dpp(0, (int)v, DPP_X2, 0xf, 0xf, true));
                v = min(v, (unsigned)__builtin_amdgcn_update_dpp(0, (int)v, DPP_HM, 0xf, 0xf, true)); }
  v = __builtin_amdgcn_readfirstlane(v);
  asm volatile("" ::: "memory");
  return v;
}
template <int SLP = 3> DEV unsigned wait_prog(Ctx& c, int kind, int h0, int nhmask, unsigned need, unsigned have) {
  if (have >= need || c.dead) return have;
  for (unsigned spins = 0;;) {
    MEMF();
    const unsigned v = prog_finish(prog_issue(c, kind, h0, nhmask), nhmask);
    if (v >= need) return v;
    __builtin_amdgcn_s_sleep(SLP);
    if (++spins > SPIN_CAP) { give_up(c, 0x100u + kind); return need; }
  }
}
DEV void publish_prog(Ctx& c, int kind, int h, unsigned val) {
  waitvm<0>();
  bar();
  if (c.wave == 0 && c.lane == 0) st4c(c.rw, pgoff(c, kind, h), OFF_CTL, val);
}

constexpr int BLK = 24576;
DEV void blk_voff(int lane, int wave, int ldb, bool perm, int (&v)[6]) {
#pragma unroll
  for (int i = 0; i < 6; ++i) {
    const int p = i * 64 + lane, ir = 16 * wave + p / 24, ch = p % 24, i32 = ir & 31;
    const int kr = 8 * (2 * (i32 >> 4) + ((i32 >> 2) & 1)) + 4 * ((i32 >> 3) & 1) + (i32 & 3);
    v[i] = ((ir & 32) + (perm ? krow(kr) : kr)) * ldb + ((((ch >> 1) ^ ((ir >> 1) & 3)) << 1) + (ch & 1)) * 16 - (i & 3) * 1024;
  }
}
DEV void blk_toff(int lane, int wave, int (&ta)[3]) {
  const int g = lane >> 4, q = (lane & 15) >> 2;
#pragma unroll
  for (int t = 0; t < 3; ++t) ta[t] = (16 * (g >> 1) + 4 * (g & 1) + q) * 384 + ((3 * wave + t) ^ (2 * (g & 1) + (q >> 1))) * 32 + (lane & 3) * 8;
}
DEV u32x4 blk_frag(LAS unsigned char* a, int half) { return cat(trread(a + half * 12288), trread(a + half * 12288 + 3072)); }

template <int SLP> DEV void watch(Ctx& c, int kind, int h, unsigned need) {
  if (c.wave == 0) {
    for (unsigned spins = 0; !c.dead;) {
      MEMF();
      if (__builtin_amdgcn_readfirstlane(ld4c(c.rw, pgoff(c, kind, h), OFF_CTL)) >= need) break;
      __builtin_amdgcn_s_sleep(SLP);
      if (++spins > SPIN_CAP) give_up(c, 0xA00u + kind);
    }
  }
  bar();
  MEMF();
}
DEV void count_up(Ctx& c, GAS unsigned* ctl, int word) {
  bar_vm();
  if (c.wave == 0 && c.lane == 0) __hip_atomic_fetch_add(ctl + word, 1u, RLX_AGENT);
}
DEV void wait_count(Ctx& c, int word, unsigned need) {
  for (unsigned spins = 0; !c.dead;) {
    MEMF();
    if (__builtin_amdgcn_readfirstlane(ld4c(c.rw, word * 4, OFF_CTL)) >= need) break;
    __builtin_amdgcn_s_sleep(8);
    if (++spins > SPIN_CAP) give_up(c, 0x600u + word);
  }
  MEMF();
}
DEV void probe_vote(unsigned char* ws, int idx) {
  unsigned char* p0 = ws + mbx_base(ws) + NSLOT * MB_SLOT * 2 + idx * 256 + (threadIdx.x & 31) * 4;
  unsigned best[2] = {0xffffffffu, 0xffffffffu};
  for (int it = 0; it < 10; ++it) {
    unsigned char* p = p0 + (it & 1) * 8192;
    unsigned long long t0, t1; const unsigned v = 0;
    asm volatile("s_memtime %0\n\ts_waitcnt lgkmcnt(0)\n\tglobal_store_dword %3, %2, off sc1\n\ts_waitcnt vmcnt(0)\n\ts_memtime %1\n\ts_waitcnt lgkmcnt(0)"
                 : "=&s"(t0), "=&s"(t1) : "v"(v), "v"(p) : "memory");
    const unsigned d = (unsigned)(t1 - t0);
    if (it & 1) best[1] = min(best[1], d); else best[0] = min(best[0], d);
  }
  const unsigned near = best[1] < best[0] ? 1u : 0u;
  if ((threadIdx.x & 63) == 0) __hip_atomic_fetch_add((GAS unsigned*)(ws + OFF_CTL) + CT_PAR, 1u + ((xcc_id() < 4 ? near : 1u - near) << 16), RLX_AGENT);
}
DEV void mbx_init(Ctx& c, int bp, const u16* mem0, unsigned mb) {
  const int tid = c.wave * 64 + c.lane, x = (bp * 256 + tid) * 16;
  static_assert(192 * 256 * 16 == NSLOT * MB_SLOT, "mailbox init");
  u32x4 v = {SENT, SENT, SENT, SENT};
  if ((x >= MB_MA && x < MB_MA + 10240) || (x >= MB_MB && x < MB_MB + 10240)) {
    const int y = x - (x >= MB_MB ? MB_MB : MB_MA), j = y >> 7, r = (y >> 4) & 7;
    v = *(const u32x4*)(mem0 + r * DM + 8 * j);
#pragma unroll
    for (int k = 0; k < 4; ++k) v[k] = nosent(v[k]);
  }
  if (x == MB_HA || x == MB_HB) v = u32x4{0u, 0u, 0u, 0u};
  st16c(c.rw, l2p(x), mb, v);
  st16c(c.rw, l2p(x), mb + 8192, v);
  st16c(c.rw, bp < 20 ? l2p(MB_RPT) + (bp * 256 + tid) * 16 : OOB, mb, u32x4{0u, 0u, 0u, 0u});
}
DEV void mbx_ready(Ctx& c) {
  wait_count(c, CT_LN, 64);
  unsigned v = 0;
  for (unsigned spins = 0; !c.dead;) {
    MEMF();
    v = __builtin_amdgcn_readfirstlane(ld4c(c.rw, CT_PAR * 4, OFF_CTL));
    if ((v & 0xffffu) >= 32u) break;
    __builtin_amdgcn_s_sleep(8);
    if (++spins > SPIN_CAP) give_up(c, 0x6ffu);
  }
  c.mb += (v >> 16) > 16u ? 8192u : 0u;
  c.flip = (v >> 16) > 16u ? -8192 : 8192;
}
DEV void mod_part(Ctx& c, int b, const u16* cvec, const u16* wmod, GAS unsigned* ctl) {
  LAS float* sc = (LAS float*)c.lds;
  LAS float* red = (LAS float*)(c.lds + 4096);
  const int tid = c.wave * 64 + c.lane, cg = tid & 7, rg = tid >> 3;
  const rsrc_t rm = mkrsrc(wmod, DM * 3840 * 2);
  u32x4 wv[20];
#pragma unroll
  for (int i = 0; i < 20; ++i) wv[i] = ld16n(rm, rg * 7680 + cg * 16, i * 32 * 7680 + b * 128);
  SB0();
  for (int i = tid; i < DM; i += 256) { const float v = bf2f(cvec[i]); sc[i] = rbf(v / (1.f + __expf(-v))); }
  bar();
  float acc[8] = {0, 0, 0, 0, 0, 0, 0, 0};
#pragma unroll
  for (int i = 0; i < 20; ++i) {
    const float s = sc[rg + 32 * i];
#pragma unroll
    for (int k = 0; k < 4; ++k) { acc[2 * k] += s * lo2f(wv[i][k]); acc[2 * k + 1] += s * hi2f(wv[i][k]); }
  }
#pragma unroll
  for (int k = 0; k < 8; ++k) red[rg * 64 + cg * 8 + k] = acc[k];
  bar();
  float s = 0.f;
#pragma unroll
  for (int r = 0; r < 32; ++r) s += red[r * 64 + c.lane];
  st4c(c.rw, tid < 64 ? tid * 4 : OOB, OFF_MOD + b * 256, fbits(rbf(s)));
  count_up(c, ctl, b < 20 ? CT_MOD : CT_MOD2);
}
DEV void load_mod(Ctx& c, float (&o)[24], int vec) {
  const int l32 = c.lane & 31;
#pragma unroll
  for (int u = 0; u < 6; ++u) {
    const u32x4 m = ld16c(c.rw, (u < 4 || l32 < 16) ? (l32 + 32 * (u >> 1)) * 32 + (u & 1) * 16 : OOB, OFF_MOD + vec * DM * 4);
#pragma unroll
    for (int k = 0; k < 4; ++k) o[u * 4 + k] = bitsf(m[k]);
  }
}
DEV void ln1_issue(Ctx& c, u32x4 (&xr)[4][3], int sg, const u16* x) {
  const int l32 = c.lane & 31, sl = c.wave * 2 + (c.lane >> 5);
  const rsrc_t rx = mkrsrc(x, NTOK * DM * 2);
#pragma unroll
  for (int i = 0; i < 4; ++i)
#pragma unroll
    for (int u = 0; u < 3; ++u)
      asm volatile("s_nop 4\n\tbuffer_load_dwordx4 %0, %1, %2, 0 offen nt" : "=v"(xr[i][u]) : "v"((sg >= 0 && (u < 2 || l32 < 16)) ? (sg * 32 + tokslot(i & 1, (i & 2) * 4 + sl)) * (DM * 2) + (l32 + 32 * u) * 16 : OOB), "s"(rx) : "memory");
}
DEV void ln1_norm(Ctx& c, u32x4 (&xr)[4][3]) {
  const bool has3 = (c.lane & 31) < 16;
#pragma unroll
  for (int i = 0; i < 4; ++i) {
    float v[24];
    pin_v(xr[i][0]); pin_v(xr[i][1]); pin_v(xr[i][2]);
#pragma unroll
    for (int u = 0; u < 3; ++u)
#pragma unroll
      for (int k = 0; k < 4; ++k) { v[u * 8 + 2 * k] = lo2f(xr[i][u][k]); v[u * 8 + 2 * k + 1] = hi2f(xr[i][u][k]); }
    ln_row(v, has3);
#pragma unroll
    for (int u = 0; u < 3; ++u) { xr[i][u] = pk8(v + u * 8); pin_v(xr[i][u]); }
  }
}
DEV void ln1_seg(Ctx& c, u32x4 (&xr)[4][3], int sg) {
  const int l32 = c.lane & 31, sl = c.wave * 2 + (c.lane >> 5);
  const bool has3 = l32 < 16;
  LAS unsigned char* stg = c.lds + 6 * BLK;
  wait_count(c, CT_MOD, 20);
  float sh[24], scl[24];
  load_mod(c, sh, 0); load_mod(c, scl, 1);
#pragma unroll
  for (int i = 0; i < 4; ++i) {
    float v[24];
#pragma unroll
    for (int u = 0; u < 3; ++u)
#pragma unroll
      for (int k = 0; k < 4; ++k) { v[u * 8 + 2 * k] = lo2f(xr[i][u][k]); v[u * 8 + 2 * k + 1] = hi2f(xr[i][u][k]); }
    modulate(v, sh, scl);
#pragma unroll
    for (int u = 0; u < 3; ++u) xr[i][u] = pk8(v + u * 8);
    if (i) bar();
#pragma unroll
    for (int u = 0; u < 3; ++u) if (u < 2 || has3) *(LAS u32x4*)(stg + (l32 + 32 * u) * 144 + sl * 16) = xr[i][u];
    bar();
#pragma unroll
    for (int k = 0; k < 3; ++k) {
      const int p = (c.wave * 3 + k) * 8 + (c.lane >> 3);
      st16c(c.rw, p < 80 ? p * 256 + (c.lane & 7) * 16 : OOB, OFF_HF + (sg * 2 + (i & 1)) * 20480 + (i & 2) * 64, *(LAS u32x4*)(stg + p * 144 + (c.lane & 7) * 16));
    }
  }
}
DEV void pre_work(Ctx& c, int h, int qt, const u16* Wv, const u16* Wo, unsigned char* ws) {
  const int lane = c.lane, wave = c.wave, g = lane >> 4, n = lane & 15;
  GAS unsigned* ctl = (GAS unsigned*)(ws + OFF_CTL);
  const rsrc_t rv = mkrsrc(Wv, DM * LDW * 2), ro = mkrsrc(Wo, LDW * DM * 2);
  int vv[12];
#pragma unroll
  for (int i = 0; i < 12; ++i) { const int p = i * 64 + lane, ir = p / 24, cp = p % 24; vv[i] = krow(tokslot(ir >> 4, ir & 15)) * (LDW * 2) + (cp ^ ((ir >> 1) & 7)) * 16 - (i & 3) * 1024; }
  auto v_issue = [&](int it) { dma_grp<12, (NTM & 4) != 0>(rv, vv, 32 * (wave + 4 * it) * (LDW * 2) + h * DH * 2, c.ldsb + 61440 + wave * 24576 + (it & 1) * 12288); };
  {
    int vo[15];
#pragma unroll
    for (int i = 0; i < 15; ++i) { const int p = i * 64 + lane, r = p / 20; vo[i] = r * (DM * 2) + ((p % 20) ^ ((r >> 2) & 2)) * 16 - (i & 3) * 1024; }
    dma_grp<15, (NTM & 8) != 0>(ro, vo, (h * DH + 48 * wave) * (DM * 2) + qt * 320, c.ldsb + wave * 15360);
  }
  v_issue(0); v_issue(1);
  u32x4 a[5][2][6];
  auto v_take = [&](int it) {
#pragma unroll
    for (int tt = 0; tt < 2; ++tt)
#pragma unroll
      for (int ks = 0; ks < 6; ++ks)
        a[it][tt][ks] = *(LAS u32x4*)(c.lds + 61440 + wave * 24576 + (it & 1) * 12288 + (16 * tt + n) * 384 + (((4 * ks + g) ^ ((n >> 1) & 7)) * 16));
#pragma unroll
    for (int tt = 0; tt < 2; ++tt)
#pragma unroll
      for (int ks = 0; ks < 6; ++ks) pin_v(a[it][tt][ks]);
  };
  const int ov = (n >> 3) * (16 * 20 * 1024) + (g * 16 + (h & 1) * 8 + (n & 7)) * 16;
  waitvm<24>();
  bar();
  MS(5);
  u32x4 Bf[10][6];
  {
    LAS unsigned char* be = c.lds + (8 * g + (n >> 2)) * 320 + (lane & 3) * 8 + (g & 1) * 32;
    LAS unsigned char* bo = c.lds + (8 * g + (n >> 2)) * 320 + (lane & 3) * 8 + (1 - (g & 1)) * 32;
#pragma unroll
    for (int t = 0; t < 10; ++t)
#pragma unroll
      for (int ks = 0; ks < 6; ++ks) { LAS unsigned char* b_ = ((t & 1) ? bo : be) + ks * 10240 + (t & ~1) * 32; Bf[t][ks] = cat(trread(b_), trread(b_ + 1280)); }
#pragma unroll
    for (int t = 0; t < 10; ++t)
#pragma unroll
      for (int ks = 0; ks < 6; ++ks) { if (t < 9) pin_a(Bf[t][ks]); else pin_v(Bf[t][ks]); }
  }
  waitvm<12>(); v_take(0); v_issue(2);
  waitvm<12>(); v_take(1); v_issue(3);
  const f32x4 z4 = {0, 0, 0, 0};
  auto mm = [&](const u32x4 (&af)[2][6], int it) __attribute__((always_inline)) {
    const int ob = OFF_WVO + ((20 * qt * 16 + (h >> 1)) * 20 + wave + 4 * it) * 1024;
    f32x4 a0[10], a1[10];
    u32x4 o;
#pragma unroll
    for (int t = 0; t <= 10; ++t) {
#pragma unroll
      for (int ks = 0; ks < 6; ++ks) {
        SB0();
        if (t < 10) a0[t] = mfma32(af[0][ks], Bf[t][ks], ks ? a0[t] : z4);
        SB0();
        if (t > 1 && ks == 0) st16c(c.rw, ov, ob + (t - 2) * (2 * 16 * 20 * 1024), o);
        if (t > 0 && ks == 4) o.x = pk2(a0[t - 1][0], a0[t - 1][1]);
        if (t > 0 && ks == 5) o.z = pk2(a1[t - 1][0], a1[t - 1][1]);
        SB0();
        if (t < 10) a1[t] = mfma32(af[1][ks], Bf[t][ks], ks ? a1[t] : z4);
        SB0();
        if (t > 0 && ks == 4) o.y = pk2(a0[t - 1][2], a0[t - 1][3]);
        if (t > 0 && ks == 5) o.w = pk2(a1[t - 1][2], a1[t - 1][3]);
      }
    }
    st16c(c.rw, ov, ob + 9 * (2 * 16 * 20 * 1024), o);
  };
  mm(a[0], 0);
  waitvm<22>();
  v_take(2); v_issue(4);
  mm(a[1], 1);
  waitvm<32>(); v_take(3);
  mm(a[2], 2);
  waitvm<20>(); v_take(4);
  mm(a[3], 3);
  mm(a[4], 4);
  MS(6);
  count_up(c, ctl, CT_WVO + 32 * qt);
}

DEV int slot_kstep(int w, int sg) {
  if (w == 3) return sg == 0 ? 9 : (sg == 9 ? 0 : sg);
  if (sg == 9) return 9;
  int gr = w + sg / 3; gr = gr >= 3 ? gr - 3 : gr;
  return 3 * gr + sg % 3;
}
constexpr bool a_inlds(int m, int sg) { return m == 1 && sg >= 4 && sg <= 8; }
constexpr int a_regidx(int m, int sg) { return m == 0 ? sg : (sg < 4 ? 10 + sg : 14); }
constexpr int A_RES = 0, A_MBUF = 5 * BLK, A_SP = A_MBUF + 12288;

DEV void role_A(Ctx& c, int h, const u16* Wq, const u16* Wk) {
  const int lane = c.lane, wave = c.wave, g = lane >> 4, n = lane & 15;
  u32x8 W[45];
  {
    const rsrc_t rq = mkrsrc(Wq, DM * LDW * 2), rk = mkrsrc(Wk, DM * LDW * 2);
    int voff[6], ta[3];
    blk_voff(lane, wave, LDW * 2, true, voff); blk_toff(lane, wave, ta);
    auto issue = [&](int m, int t, int b) { dma_grp<6, (NTM & 1) != 0>(m ? rk : rq, voff, t * 64 * (LDW * 2) + h * DH * 2, c.ldsb + b * BLK + wave * 6144); };
    u32x8 keep[2][3];
#define A_TAKE(M, P, B0, B1, B2, B3) { \
      _Pragma("unroll") for (int e = 0; e < (P == 0 ? 4 : 3); ++e) { \
        const int sg = e < 3 ? 3 * e + P : 9, t = slot_kstep(wave, sg), gi = t == 9 ? 3 : t / 3; \
        LAS unsigned char* bb = c.lds + (gi == 0 ? B0 : (gi == 1 ? B1 : (gi == 2 ? B2 : B3))) * BLK; \
        _Pragma("unroll") for (int tl = 0; tl < 3; ++tl) { \
          const u32x8 f_ = cat8(blk_frag(bb + ta[tl], 0), blk_frag(bb + ta[tl], 1)); \
          if (a_inlds(M, sg)) keep[e - 1][tl] = f_; else W[a_regidx(M, sg) * 3 + tl] = f_; } \
        _Pragma("unroll") for (int tl = 0; tl < 3; ++tl) { \
          if (a_inlds(M, sg)) pin_v(keep[e - 1][tl]); else if (M == 0) pin_a(W[a_regidx(M, sg) * 3 + tl]); else pin_v(W[a_regidx(M, sg) * 3 + tl]); } } \
      bar(); }
#define A_PUT(E, SG) { _Pragma("unroll") for (int tl = 0; tl < 3; ++tl) { LAS unsigned char* d_ = c.lds + (SG - 4) * BLK + wave * 6144 + tl * 2048 + lane * 16; \
        *(LAS u32x4*)d_ = u32x4{keep[E][tl][0], keep[E][tl][1], keep[E][tl][2], keep[E][tl][3]}; *(LAS u32x4*)(d_ + 1024) = u32x4{keep[E][tl][4], keep[E][tl][5], keep[E][tl][6], keep[E][tl][7]}; } }
    waitvm<0>();
    issue(0, 0, 0); issue(0, 3, 1); issue(0, 6, 2); issue(0, 9, 3); issue(0, 1, 4); issue(0, 4, 5);
    waitvm<12>(); bar(); A_TAKE(0, 0, 0, 1, 2, 3)
    issue(0, 7, 0); issue(0, 2, 1); issue(0, 5, 2); issue(0, 8, 3);
    waitvm<18>(); bar(); A_TAKE(0, 1, 4, 5, 0, 0)
    issue(1, 0, 4); issue(1, 3, 5); issue(1, 6, 0);
    waitvm<18>(); bar(); A_TAKE(0, 2, 1, 2, 3, 0)
    issue(1, 9, 1); issue(1, 1, 3);
    waitvm<6>(); bar(); A_TAKE(1, 0, 4, 5, 0, 1)
    issue(1, 4, 4); issue(1, 7, 5); issue(1, 2, 1);
    A_PUT(1, 6)
    waitvm<6>(); bar(); A_TAKE(1, 1, 3, 4, 5, 0)
    issue(1, 5, 4); issue(1, 8, 5);
    A_PUT(0, 4) A_PUT(1, 7)
    waitvm<0>(); bar(); A_TAKE(1, 2, 1, 4, 5, 0)
    A_PUT(0, 5) A_PUT(1, 8)
#undef A_TAKE
#undef A_PUT
  }
  MS(2);
  LAS unsigned char* resw = c.lds + A_RES + wave * 6144 + lane * 16;
  LAS unsigned char* resw3 = resw + 3 * BLK;
  const int idx = (lane & 8) ? 0xEEEE : 0x4444;
  const int r7 = (n & 7) - 4 * (g & 1);
  const u32x2 sel = {(r7 == 0 ? 0x3F80u : 0u) | (r7 == 1 ? 0x3F800000u : 0u), (r7 == 2 ? 0x3F80u : 0u) | (r7 == 3 ? 0x3F800000u : 0u)};
  const u32x4 ident = n < 8 ? u32x4{sel.x, sel.y, 0u, 0u} : u32x4{0u, 0u, sel.x, sel.y};
  LAS unsigned char* mwr = c.lds + A_MBUF + 3 * wave * 1024 + lane * 16;
  LAS unsigned char* mrA = c.lds + A_MBUF + slot_kstep(wave, 3) * 1024 + lane * 16;
  LAS unsigned char* mrB = c.lds + A_MBUF + slot_kstep(wave, 6) * 1024 + lane * 16;
  LAS unsigned char* mrC = c.lds + A_MBUF + slot_kstep(wave, 9) * 1024 + lane * 16;
  int moff[3];
#pragma unroll
  for (int sg = 0; sg < 3; ++sg) moff[sg] = l2p(MB_MA + slot_kstep(wave, sg) * 1024);
  int l16 = lane * 16;
  pin_v(l16);
  const int toff = lane < 24 ? l2p(MB_MA + slot_kstep(wave, lane >> 3) * 1024 + (lane & 7) * 128) : OOB;
  const int qi = lane >> 5, cc = lane & 31, row = 2 * wave + qi;
  const int poff = cc < 20 ? p_head(h) + (cc >> 2) * 128 + row * 16 + (cc & 3) * 4 : OOB;
  const int kmoff = n >= 8 ? (g * 8 + n - 8) * 8 : OOB / 2;
  LAS unsigned char* spw = c.lds + A_SP + (n < 8 ? wave * 2176 : 4 * 2176) + (n & 7) * 272 + g * 32;
  LAS unsigned char* spm = c.lds + A_SP + ((n < 8 && g >= 2) ? wave * 2176 : 4 * 2176 + (n >> 3) * 64 + (g >> 1) * 32) + (n & 7) * 272 + (g & 1) * 16;
  LAS unsigned char* spr = c.lds + A_SP + row * 272 + cc * 8;
  for (int i = lane; i < 8 * 24; i += 64) *(LAS float*)(c.lds + A_SP + wave * 2176 + (i / 24) * 272 + (40 + i % 24) * 4) = -INFINITY;
  int fko = pgoff(c, PG_K, h);
  pin_v(fko);
  unsigned haveK = wait_prog(c, PG_K, h, 0, 1, 0);
  u32x4 kx[3];
#pragma unroll
  for (int i = 0; i < 3; ++i) kx[i] = ld16c(c.rw, lane * 16 + i * 1024, OFF_KX + (h * 4 + wave) * 3072);
  mbx_ready(c);
  const int fkm = (wave == 0 && lane == 0) ? pgoff(c, PG_KM, h) : OOB;
  MS(3);
  unsigned SA = rtnow();
  bool late = false;
  u32x6 TZ = {0u, 0u, 0u, 0u, 0u, 0u};
  Pace pace = {0u, SENT, 1.f / 21.f};
  pin_v(resw); pin_v(resw3); pin_v(spw); pin_v(spm); pin_v(mwr); pin_v(mrA); pin_v(mrB); pin_v(mrC);
  auto step = [&](int s, auto LASTC) {
    constexpr bool LAST = decltype(LASTC)::v;
    int slot = c.mb + (s & 7) * (MB_SLOT * 2), kb = OFF_KX + (((s + 1) * NH + h) * 4 + wave) * 3072;
    pin_s(slot);
    unsigned fK, touch;
    u32x2 hd;
    u32x4 T01;
    unsigned spins = 0, twake;
    int d0;
    {
#define A_RA(L) ((L) < 3 ? resw + (L) * BLK : resw3 + ((L) - 3) * BLK)
      u32x4 stl[5], sth[5], mf[10];
      f32x4 aq[3], ak[3];
#pragma unroll
      for (int L = 0; L < 3; ++L) { stl[L] = *(LAS u32x4*)A_RA(L); sth[L] = *(LAS u32x4*)(A_RA(L) + 1024); }
      u32x8 sa[2];
#pragma unroll
      for (int L = 0; L < 2; ++L) { sa[L] = cat8(*(LAS u32x4*)A_RA(3 + L), *(LAS u32x4*)(A_RA(3 + L) + 1024)); pin_a(sa[L]); }
      d0 = rtdiff(SA, rtnow());
      int mslot = late ? slot + MB_3RD : slot;
      unsigned Tw = late ? SA + c.pD + 41 : SA, m, lim;
      touch = 0;
      spins = late ? 2 : 0;
#define A_OP(P, M_, SG) { f32x4& acc_ = M_ ? ak[P] : aq[P]; \
        if constexpr (a_inlds(M_, SG) && P == 0 && SG >= 7) acc_ = smfma(mf[SG], sa[SG >= 7 ? SG - 7 : 0], acc_, idx); \
        else if constexpr (a_inlds(M_, SG)) acc_ = smfma(mf[SG], cat8(stl[SG - 4], sth[SG - 4]), acc_, idx); \
        else acc_ = smfma(mf[SG], W[a_regidx(M_, SG) * 3 + P], acc_, idx); SB0(); }
#define A_RL(L, P) { stl[L] = *(LAS u32x4*)(A_RA(L) + P * 2048); SB0(); }
#define A_RH(L, P) { sth[L] = *(LAS u32x4*)(A_RA(L) + P * 2048 + 1024); SB0(); }
#define A_MW(SG) { *(LAS u32x4*)(mwr + SG * 1024) = mf[SG]; SB0(); }
#define A_MR(SG) { mf[SG] = *(LAS u32x4*)((SG < 6 ? mrA : (SG < 9 ? mrB : mrC)) + (SG % 3) * 1024); SB0(); }
#define A_CV(Q, I) { X[I] = I < 2 ? pk2(aq[Q][2 * (I & 1)], aq[Q][2 * (I & 1) + 1]) : pk2(ak[Q][2 * (I & 1)], ak[Q][2 * (I & 1) + 1]); SB0(); }
#define A_WL(N) { __builtin_amdgcn_s_waitcnt(0xC07F | ((N) << 8)); SB0(); }
      do {
        if (spins == 1 && __builtin_amdgcn_ballot_w64(touch == SENT) != 0ull) { mslot = slot + MB_3RD; Tw = SA + c.pD + 20; spins = 2; }
        int mso[3] = {mslot + moff[0], mslot + moff[1], mslot + moff[2]};
        pin_s(mso[0]); pin_s(mso[1]); pin_s(mso[2]);
        if (WFINE && spins == 0 && d0 >= WF_MIN && d0 < 2000) wait_fine(Tw, d0); else wait_raw(Tw + WOFF);
#pragma unroll
        for (int t = 0; t < 3; ++t) {
          mf[t] = ld16c(c.rw, l16, mso[t]);
          SB0();
          if (t == 0) twake = cyc();
          aq[t] = __builtin_bit_cast(f32x4, cat(imm2<0>(), imm2<0>())); ak[t] = __builtin_bit_cast(f32x4, cat(imm2<0>(), imm2<0>())); pin_v(aq[t]); pin_v(ak[t]);
          if (t < 2) asm volatile("s_nop 5");
          SB0();
        }
        __builtin_amdgcn_s_waitcnt(0xC07F);
        lim = (c.dead || spins > SPIN_CAP) ? SENT : SENT - 1u;
        mslot = slot + (spins == 0 ? MB_2ND : MB_3RD);
        Tw = spins == 0 ? SA : (spins == 1 ? SA + c.pD + 20 : Tw + 45);
        ++spins;
        pin_v(lim); pin_s(mslot); pin_s(Tw); pin_s(spins);
        __builtin_amdgcn_s_sleep(PFA);
        SB0();
        touch = ld4c(c.rw, spins == 1 ? toff : OOB, mslot);
        SB0();
        A_OP(0, 0, 0) A_MW(0) A_OP(0, 1, 0) m = umax3(mf[0].x, mf[0].y, mf[0].z); SB0();
        A_OP(1, 0, 0) A_OP(1, 1, 0) A_MW(1) A_OP(2, 0, 0) m = umax3(m, mf[0].w, mf[1].x); m = umax3(m, mf[1].y, mf[1].z); SB0();
        A_OP(2, 1, 0) A_OP(0, 0, 1) A_MW(2) A_OP(0, 1, 1) m = umax3(m, mf[1].w, mf[2].x); m = umax3(m, mf[2].y, mf[2].z); SB0();
        A_OP(1, 0, 1)
        m = max(m, mf[2].w); SB0();
        A_OP(1, 1, 1)
      } while (__builtin_expect(__builtin_amdgcn_ballot_w64(m > lim) != 0ull, 0));
      A_OP(2, 0, 1)
      asm volatile("s_waitcnt vmcnt(1) lgkmcnt(0)\n\ts_barrier" ::: "memory");
      SB0();
      A_MR(3) A_OP(2, 1, 1) A_MR(4) A_OP(0, 0, 2) A_MR(5) A_OP(0, 1, 2) A_MR(6) A_OP(1, 0, 2) A_MR(7) A_OP(1, 1, 2) A_MR(8) A_OP(2, 0, 2) A_MR(9) A_OP(2, 1, 2)
      f32x4 tr0, tr1, tr2;
      u32x4 X;
      A_WL(5) A_OP(0, 1, 3) if (FLG == 2) st4c(c.rw, fkm, OFF_CTL, (unsigned)s); else fK = ld4c(c.rw, fko, OFF_CTL); SB0(); A_OP(0, 0, 3) hd = ld8c(c.rw, l2p(MB_HA), slot); SB0();
      A_OP(0, 1, 4) A_RL(0, 1) A_OP(0, 0, 4) A_RH(0, 1) A_WL(2) A_OP(0, 1, 5) A_RL(1, 1) A_OP(0, 0, 5) A_RH(1, 1) A_OP(0, 1, 6) A_RL(2, 1) A_OP(0, 0, 6) A_RH(2, 1)
      A_OP(0, 1, 7) A_RL(3, 1) A_OP(0, 0, 7) A_RH(3, 1) A_OP(0, 1, 8) A_RL(4, 1) A_OP(0, 0, 8) A_RH(4, 1) A_OP(0, 1, 9) if (FLG == 0) st4c(c.rw, fkm, OFF_CTL, (unsigned)s); if (FLG == 2) fK = ld4c(c.rw, fko, OFF_CTL); SB0(); A_OP(0, 0, 9)
      A_OP(1, 1, 3)
      A_OP(1, 0, 3) A_WL(4) A_OP(1, 1, 4) A_RL(0, 2) A_OP(1, 0, 4) A_RH(0, 2) A_OP(1, 1, 5) A_RL(1, 2) A_OP(1, 0, 5) A_RH(1, 2)
      A_OP(1, 1, 6) A_RL(2, 2) A_OP(1, 0, 6) A_RH(2, 2) A_WL(6) A_OP(1, 1, 7) A_RL(3, 2) A_OP(1, 0, 7) A_RH(3, 2) A_OP(1, 1, 8) A_RL(4, 2)
      A_OP(1, 0, 8) A_RH(4, 2) A_OP(1, 1, 9) A_CV(0, 0) A_CV(0, 1) A_OP(1, 0, 9) A_CV(0, 2) A_CV(0, 3)
      A_OP(2, 1, 3) A_OP(2, 0, 3) A_WL(4)
      tr0 = mfma32v(X, ident); SB0();
      A_OP(2, 1, 4) A_OP(2, 0, 4) A_CV(1, 2) A_CV(1, 3) A_OP(2, 1, 5) A_CV(1, 0) A_CV(1, 1) A_OP(2, 0, 5) A_OP(2, 1, 6)
      tr1 = mfma32v(X, ident); SB0();
      A_OP(2, 0, 6) A_WL(0) A_OP(2, 1, 7) T01.x = pk2(tr0[0], tr0[1]); T01.y = pk2(tr0[2], tr0[3]); SB0();
      A_OP(2, 0, 7) A_OP(2, 1, 8) A_OP(2, 0, 8) T01.z = pk2(tr1[0], tr1[1]); T01.w = pk2(tr1[2], tr1[3]); SB0();
      A_OP(2, 1, 9) A_OP(2, 0, 9)
      const f32x4 z4 = {0, 0, 0, 0};
      f32x4 sx0 = mfma32(kx[0], T01, z4), sx1 = mfma32(kx[1], T01, z4), smm = mfma32(T01, T01, z4); SB0();
      A_CV(2, 2) A_CV(2, 3) A_CV(2, 0) A_CV(2, 1)
      asm volatile("s_nop 1\n\tv_mfma_f32_16x16x32_bf16 %0, %1, %2, 0\n\ts_nop 7" : "=&v"(tr2) : "v"(X), "v"(ident));
      TZ[2] = pk2(tr2[0], tr2[1]); TZ[3] = pk2(tr2[2], tr2[3]); pin_v(TZ); SB0();
      const u32x4 T2a = __builtin_shufflevector(TZ, TZ, 2, 3, 4, 5), T2b = __builtin_shufflevector(TZ, TZ, 0, 1, 2, 3);
      sx0 = mfma32(kx[2], T2a, sx0);
      smm = mfma32(T2a, T2a, smm);
      sx1 = mfma32(kx[2], T2b, sx1);
      SB0();
#undef A_OP
#undef A_RL
#undef A_RH
#undef A_MW
#undef A_MR
#undef A_CV
#undef A_WL
#undef A_RA
      *(LAS f32x4*)(spw + 32) = sx0;
      *(LAS f32x4*)spm = smm;
      *(LAS f32x4*)(spw + 48) = sx1;
      bar();
      if constexpr (!LAST) {
        const f32x2 p0 = *(LAS f32x2*)spr, p1 = *(LAS f32x2*)(spr + 2176), p2 = *(LAS f32x2*)(spr + 4352), p3 = *(LAS f32x2*)(spr + 6528);
        const f32x2 sum = (p0 + p1) + (p2 + p3);
        const unsigned r1 = pk2(sum[0], sum[1]);
        const unsigned r2 = pk2(lo2f(r1) * SCALE, hi2f(r1) * SCALE);
        const float s0 = lo2f(r2), s1 = hi2f(r2);
        const float ml = max32a(s0, s1) * LOG2E;
        float e0 = __builtin_amdgcn_exp2f(__builtin_fmaf(s0, LOG2E, -ml)), e1 = __builtin_amdgcn_exp2f(__builtin_fmaf(s1, LOG2E, -ml));
        asm volatile("s_nop 0" : "+v"(e0), "+v"(e1));
        const float inv = __builtin_amdgcn_rcpf(sum32a(e0, e1));
        const unsigned pw = nosent(pk2(e0 * inv, e1 * inv));
        st4c(c.rw, poff, slot, pw);
        SB0();
        st4c(c.rw, poff, slot + MB_2ND, pw);
      }
    }
    SB0();
    const unsigned tdone = cyc();
    SB0();
    pin_v(touch);
    if (spins > SPIN_CAP) give_up(c, 0x200u + s);
    unsigned rep = 0;
    if constexpr (!LAST) {
      const unsigned tpub = rtnow();
      rep = pace_report(pace, s, SA, twake, tdone, spins != 1, d0);
      haveK = prog_finish(fK, 0);
      unsigned hx = __builtin_amdgcn_readfirstlane(hd.x), hy = __builtin_amdgcn_readfirstlane(hd.y);
      if (max(hx, hy) == SENT) { MEMF(); hd = ld8c(c.rw, l2p(MB_HA), slot); hx = __builtin_amdgcn_readfirstlane(hd.x); hy = __builtin_amdgcn_readfirstlane(hd.y); }
      SA = hy;
      late = rtdiff(tpub, hx) > 4;
      if (s == 0 || max(hx, hy) == SENT) { SA = tpub; late = false; }
      haveK = wait_prog(c, PG_K, h, 0, s + 2, haveK);
      SB0();
    }
    if (FLG == 1) st4c(c.rw, fkm, OFF_CTL, (unsigned)s);
    st16c(c.rw, kmoff * 2, xblk(c, s) + km_piece(h * 4 + wave), T01);
    st8c(c.rw, kmoff + 512, xblk(c, s) + km_piece(h * 4 + wave), u32x2{TZ[2], TZ[3]});
    if constexpr (!LAST) {
      pin_v(l16);
#pragma unroll
      for (int i = 0; i < 3; ++i) kx[i] = ld16c(c.rw, l16 + i * 1024, kb);
      st4c(c.rw, lane == 0 ? l2p(MB_RPT + h * 128 + wave * 4) : OOB, c.mb, rep);
    } else publish_prog(c, PG_KM, h, NSEG);
  };
  for (int s = 0; s < NSEG - 1; ++s) step(s, BoolC<false>{});
  step(NSEG - 1, BoolC<true>{});
}

constexpr int NRING = LAG + 1;
constexpr int B_RES = 0, B_RING = 32768, B_MBUF = B_RING + NRING * 20480, B_RED = B_MBUF + 12288;
constexpr int B_FLAG = B_RED + 5 * 2560;
static_assert(B_FLAG + 16 <= REC_LDS, "role B LDS");
DEV u32x2 b_reduce_load(LAS unsigned char* red, int lane) {
  const int o = lane >> 2, pp = lane & 3;
  f32x4 v = *(LAS f32x4*)(red + pp * 320 + o * 16) + *(LAS f32x4*)(red + (pp + 4) * 320 + o * 16);
  float a = v[0], b = v[1], c = v[2], d = v[3];
  asm volatile("s_nop 1\n\t"
               "v_add_f32_dpp %0, %0, %0 quad_perm:[1,0,3,2] row_mask:0xf bank_mask:0xf\n\tv_add_f32_dpp %1, %1, %1 quad_perm:[1,0,3,2] row_mask:0xf bank_mask:0xf\n\t"
               "v_add_f32_dpp %2, %2, %2 quad_perm:[1,0,3,2] row_mask:0xf bank_mask:0xf\n\tv_add_f32_dpp %3, %3, %3 quad_perm:[1,0,3,2] row_mask:0xf bank_mask:0xf\n\t"
               "v_add_f32_dpp %0, %0, %0 quad_perm:[2,3,0,1] row_mask:0xf bank_mask:0xf\n\tv_add_f32_dpp %1, %1, %1 quad_perm:[2,3,0,1] row_mask:0xf bank_mask:0xf\n\t"
               "v_add_f32_dpp %2, %2, %2 quad_perm:[2,3,0,1] row_mask:0xf bank_mask:0xf\n\tv_add_f32_dpp %3, %3, %3 quad_perm:[2,3,0,1] row_mask:0xf bank_mask:0xf"
               : "+v"(a), "+v"(b), "+v"(c), "+v"(d));
  return u32x2{pk2(a, b), pk2(c, d)};
}
DEV void b_px_issue(Ctx& c, int sx, u32x4 (&px)[4][5]) {
  const int pv = p_head(8 * c.wave + (c.lane >> 3)) + (c.lane & 7) * 16;
#pragma unroll
  for (int qg = 0; qg < 4; ++qg)
#pragma unroll
    for (int f = 0; f < 5; ++f)
      if (f <= qg + 1) px[qg][f] = ld16c(c.rw, pv + f * 128, xblk(c, sx) + qg * 49152);
}
DEV void b_outx_mma(LAS unsigned char* redw, const u32x4 (&z)[5], const u32x4 (&px)[4][5]) {
  f32x4 d[4];
#pragma unroll
  for (int qg = 0; qg < 4; ++qg) d[qg] = f32x4{0, 0, 0, 0};
#pragma unroll
  for (int f = 0; f < 5; ++f)
#pragma unroll
    for (int qg = 0; qg < 4; ++qg)
      if (f <= qg + 1) d[qg] = mfma32(z[f], px[qg][f], d[qg]);
  SB0();
#pragma unroll
  for (int qg = 0; qg < 4; ++qg) *(LAS f32x4*)(redw + (1 + qg) * 2560) = d[qg];
}
DEV void b_outx_fin(Ctx& c, int j, int sx, int aoff) {
  const u32x2 o = b_reduce_load(c.lds + B_RED + (1 + c.wave) * 2560, c.lane);
  st8c(c.rw, aoff, xblk(c, sx) + XB_AO + l2p(j * 512), o);
}

DEV void role_B(Ctx& c, int j) {
  const int lane = c.lane, wave = c.wave;
  int tf[10];
#pragma unroll
  for (int sg = 0; sg < 10; ++sg) tf[sg] = slot_kstep(wave, sg);
  u32x8 W[4][9];
  const int wbase = OFF_WVO + (j * 16 + wave * 4) * 20 * 1024;
  wait_count(c, CT_WVO + 32 * (j / 20), NH);
  MS(1);
  {
    u32x4 t9[8];
#pragma unroll
    for (int i = 0; i < 8; ++i) t9[i] = ld16c(c.rw, lane * 16, wbase + ((i >> 1) * 20 + tf[9] * 2 + (i & 1)) * 1024);
#pragma unroll
    for (int tau = 0; tau < 4; ++tau)
#pragma unroll
      for (int sg = 0; sg < 9; ++sg)
        W[tau][sg] = cat8(ld16c(c.rw, lane * 16, wbase + (tau * 20 + tf[sg] * 2) * 1024), ld16c(c.rw, lane * 16, wbase + (tau * 20 + tf[sg] * 2 + 1) * 1024));
#pragma unroll
    for (int i = 0; i < 8; ++i) *(LAS u32x4*)(c.lds + B_RES + wave * 8192 + i * 1024 + lane * 16) = t9[i];
  }
#pragma unroll
  for (int tau = 0; tau < 4; ++tau)
#pragma unroll
    for (int sg = 0; sg < 7; ++sg) pin_a(W[tau][sg]);
  MS(2);
  LAS unsigned char* resw = c.lds + B_RES + wave * 8192 + lane * 16;
  const int idx = (lane & 8) ? 0xEEEE : 0x4444;
  int moff[3], l16 = lane * 16, poff = p_head(8 * wave + (lane >> 3)) + (lane & 7) * 16;
  pin_v(l16); pin_v(poff);
#pragma unroll
  for (int sg = 0; sg < 3; ++sg) moff[sg] = l2p(MB_MB + tf[sg] * 1024);
  const int ooff = wave != 1 ? l2p(((lane & 2) ? ((lane & 1) ? MB_M3 : MB_M2) : ((lane & 1) ? MB_MB : MB_MA)) + j * 128 + (lane >> 2) * 8)
                             : (lane < 2 ? l2p(lane ? MB_HB : MB_HA) : OOB);
  const int rpw = (wave == 2 && lane == 0) ? l2p(MB_RPT + (32 + j) * 128) : OOB;
  const int poff0 = wave == 0 ? ooff : OOB;
  LAS unsigned char* redw = c.lds + B_RED + ((lane >> 5) == ((lane >> 3) & 1) ? (wave * 2 + ((lane >> 3) & 1)) * 320 + ((lane & 7) * 2 + ((lane >> 4) & 1)) * 16
                                                                             : ((lane & 31) >> 2) * 320 + 256 + (lane & 3) * 16);
  int uv = (lane >> 3) * 512 + (lane & 7) * 16;
  pin_v(uv);
  const int aoff = (lane & 3) == 0 ? (8 * wave) * 16 + (lane >> 2) * 8 : OOB;
  const int fcl = (lane >> 3) % 3;
  const int fa = fcl == 0 ? 3 : 2 - LAG, fb = LAG + fcl, fm = fcl == 0 ? 0 : 1;
  const int rpoff = (wave == 1 && j == 0) ? l2p(MB_RPT + 127 * 128) : OOB;
  LAS unsigned char* mwr = c.lds + B_MBUF + 3 * wave * 1024 + lane * 16;
  LAS unsigned char* mrA = c.lds + B_MBUF + tf[3] * 1024 + lane * 16;
  LAS unsigned char* mrB = c.lds + B_MBUF + tf[6] * 1024 + lane * 16;
  LAS unsigned char* mrC = c.lds + B_MBUF + tf[9] * 1024 + lane * 16;
  wait_prog(c, PG_U, 8 * wave, 7, 1, 0);
  u32x4 zn[4];
#pragma unroll
  for (int f = 0; f < 4; ++f) zn[f] = ld16c(c.rw, uv + f * 128, OFF_U + (j * NH + 8 * wave) * 512);
  *(LAS int*)(c.lds + B_FLAG) = 0; *(LAS int*)(c.lds + B_FLAG + 4) = 0;
  int fv = 0;
  Pace pace = {0u, SENT, 1.f / 21.f};
  bool pBad = true;
  unsigned pS = 0, pW = 0, pD = 0;
  int pL = 0;
  const bool master = wave == 1 && j == 0;
  mbx_ready(c);
  const int fao = (wave == 3 && lane == 0) ? pgoff(c, PG_AO, j) : OOB;
  const int foff = pgoff(c, (fcl == 0 ? PG_U : PG_PX + fcl - 1), 8 * wave + (lane & 7));
  MS(3);
  u32x4 m3[3];
#pragma unroll
  for (int sg = 0; sg < 3; ++sg) m3[sg] = ld16c(c.rw, l16, c.mb + moff[sg]);
  unsigned SA = rtnow();
  int rs = 0;
  auto step = [&](int s, auto LASTC) {
    constexpr bool LAST = decltype(LASTC)::v;
    const int slot = c.mb + (s & 7) * (MB_SLOT * 2);
    LAS unsigned char* ring = c.lds + B_RING + wave * 5120 + lane * 16;
    const int rs1 = rs + 1 == NRING ? 0 : rs + 1;
    const int sx = s >= LAG ? s - LAG : 0;
    LAS unsigned char* ringw = ring + rs * 20480;
    u32x4 mf[10], px[4][5], z[5], zo[5];
    u32x2 hd, ao, zl[4], zh[4];
    unsigned repk = 0;
    f32x4 y[4];
    const int pxb = s >= LAG ? xblk(c, sx) : OFF_WVO + j * 327680;
#define B_LD(I) { SB0(); constexpr int qg_ = I < 2 ? 0 : (I < 5 ? 1 : (I < 9 ? 2 : 3)), f_ = I - (qg_ == 0 ? 0 : (qg_ == 1 ? 2 : (qg_ == 2 ? 5 : 9))); \
                  pin_v(poff); px[qg_][f_] = ld16c(c.rw, poff + f_ * 128, pxb + qg_ * 49152); SB0(); }
#define B_S(SG, TAU) { y[TAU] = smfma(mf[SG], W[TAU][SG], y[TAU], idx); SB0(); }
#define B_MR(SG) { mf[SG] = *(LAS u32x4*)((SG < 6 ? mrA : (SG < 9 ? mrB : mrC)) + (SG % 3) * 1024); SB0(); }
#define B_WR(I) { if constexpr (I & 1) wh[I >> 1] = *(LAS u32x4*)(resw + I * 1024); else wl[I >> 1] = *(LAS u32x4*)(resw + I * 1024); SB0(); }
#define B_SL() __builtin_amdgcn_s_sleep(1);
#pragma unroll
    for (int sg = 0; sg < 3; ++sg) mf[sg] = m3[sg];
    B_LD(0) B_LD(1)
#pragma unroll
    for (int tau = 0; tau < 4; ++tau) { y[tau] = f32x4{0, 0, 0, 0}; pin_v(y[tau]); }
    B_LD(2)
    B_LD(3) B_LD(4) B_LD(5) B_LD(6)
    if (__builtin_expect(!all_valid(umax3(mx4(mf[0]), mx4(mf[1]), mx4(mf[2]))), 0)) {
      for (unsigned spins = 0; !c.dead;) {
        if (!wait_rt(SA + c.pD + 30)) __builtin_amdgcn_s_sleep(4);
        MEMF();
#pragma unroll
        for (int sg = 0; sg < 3; ++sg) mf[sg] = ld16c(c.rw, l16, slot + moff[sg]);
        if (all_valid(umax3(mx4(mf[0]), mx4(mf[1]), mx4(mf[2])))) break;
        if (++spins > SPIN_CAP) give_up(c, 0x300u + s);
      }
    }
#pragma unroll
    for (int sg = 0; sg < 3; ++sg) *(LAS u32x4*)(mwr + sg * 1024) = mf[sg];
    SB0();
    B_S(0, 0) hd = ld8c(c.rw, l2p(MB_HB), slot); B_LD(7) B_S(0, 1) B_S(0, 2) B_S(0, 3) B_LD(8)
    bar();
    SB0();
    u32x4 wl[4], wh[4];
    B_MR(3) B_S(1, 0) B_MR(4) B_S(1, 1) B_MR(5) B_S(1, 2) B_MR(6) B_S(1, 3) B_LD(9)
    B_S(2, 0) B_MR(7) B_S(2, 1) B_MR(8) B_S(2, 2) B_MR(9) B_S(2, 3) B_LD(10)
    B_S(3, 0) B_S(3, 1) B_S(3, 2) B_S(3, 3) B_LD(11)
    __builtin_amdgcn_s_waitcnt(0xC07F); SB0();
    B_S(4, 0) B_WR(0) B_S(4, 1) B_WR(1) B_S(4, 2) B_WR(2) B_S(4, 3) B_LD(12)
    B_S(5, 0) B_WR(3) B_S(5, 1) B_WR(4) B_S(5, 2) B_WR(5) B_S(5, 3) B_LD(13)
    B_S(6, 0) B_WR(6) B_S(6, 1) B_WR(7) B_S(6, 2) B_S(6, 3) fv = (int)ld4c(c.rw, foff, OFF_CTL); SB0();
    B_S(7, 0) const unsigned par = ld4c(c.rw, rpoff, c.mb); SB0(); B_S(7, 1) B_S(7, 2) B_S(7, 3)
    __builtin_amdgcn_s_waitcnt(0xC07F); SB0();
#define B_ZC(F) { asm volatile("v_mov_b64 %0, %2\n\tv_mov_b64 %1, %3" : "=&v"(zl[F]), "=&v"(zh[F]) : "v"(u32x2{zn[F].x, zn[F].y}), "v"(u32x2{zn[F].z, zn[F].w})); SB0(); }
    B_S(8, 0) B_ZC(0) B_S(8, 2) B_ZC(1) B_S(8, 1) B_ZC(2) B_S(8, 3) B_ZC(3)
#undef B_ZC
    y[0] = smfma(mf[9], cat8(wl[0], wh[0]), y[0], idx); y[2] = smfma(mf[9], cat8(wl[2], wh[2]), y[2], idx);
    y[1] = smfma(mf[9], cat8(wl[1], wh[1]), y[1], idx); y[3] = smfma(mf[9], cat8(wl[3], wh[3]), y[3], idx);

#undef B_LD
#undef B_S
#undef B_MR
#undef B_WR
#undef B_SL
    {
      const u32x2 X = pk4(fold4(y[0], y[2])), X2 = pk4(fold4(y[1], y[3]));
      auto r0 = __builtin_amdgcn_permlane16_swap(X.x, X2.x, false, false);
      auto r1 = __builtin_amdgcn_permlane16_swap(X.y, X2.y, false, false);
      z[0] = u32x4{r0[0], r1[0], r0[1], r1[1]};
    }
#pragma unroll
    for (int f = 0; f < 4; ++f) z[1 + f] = cat(zl[f], zh[f]);
    unsigned SBt = __builtin_amdgcn_readfirstlane(hd.x), SAn = __builtin_amdgcn_readfirstlane(hd.y);
    if (SBt == SENT || SAn == SENT) { MEMF(); hd = ld8c(c.rw, l2p(MB_HB), slot); SBt = __builtin_amdgcn_readfirstlane(hd.x); SAn = __builtin_amdgcn_readfirstlane(hd.y); }
    if (s == 0 || SBt == SENT || SAn == SENT) { SBt = rtnow(); SAn = SBt; }
    auto ring_io = [&]() {
#pragma unroll
      for (int f = 0; f < 5; ++f) *(LAS u32x4*)(ringw + f * 1024) = z[f];
#pragma unroll
      for (int f = 0; f < 5; ++f) zo[f] = *(LAS u32x4*)(ring + rs1 * 20480 + f * 1024);
      ao = b_reduce_load(c.lds + B_RED + (1 + wave) * 2560, lane);
    };
    if constexpr (LAST) { ring_io(); waitvm<9>(); bar(); st4c(c.rw, fao, OFF_CTL, (unsigned)(s - LAG - 1)); }
    if constexpr (!LAST) {
      u32x4 pf[5];
      f32x4 d;
      int ub = OFF_U + ((((s + 1) & UR) * NBC + j) * NH + 8 * wave) * 512;
      pin_s(ub);
      int so1 = c.mb + ((s + 1) & 7) * (MB_SLOT * 2);
      pin_s(so1);
      if (s == 0) wait_prog(c, PG_U, 8 * wave, 7, 2, 0);
      SB0();
      const int d0 = rtdiff(SBt, rtnow());
      if (WFINE && d0 >= WF_MIN && d0 < 2000) wait_fine(SBt, d0); else if (d0 > 4 - WOFF) wait_raw(SBt + WOFF);
      unsigned spins = 0, twake;
      pin_v(poff); pin_v(uv);
      MEMF();
#pragma unroll
      for (int f = 0; f < 5; ++f) { pf[f] = ld16c(c.rw, poff + f * 128, slot); SB0(); if (f == 0) twake = cyc(); SB0(); }
      __builtin_amdgcn_s_waitcnt(0xC07F);
      SB0();
      ring_io();
      SB0();
#pragma unroll
      for (int f = 0; f < 4; ++f) zn[f] = ld16c(c.rw, uv + f * 128, ub);
      SB0();
      d = f32x4{0, 0, 0, 0};
#pragma unroll
      for (int f = 0; f < 5; ++f) d = mfma32(z[f], pf[f], d);
      *(LAS f32x4*)redw = d;
      if (__builtin_expect(__builtin_amdgcn_ballot_w64(d[0] != d[0]) != 0ull, 0)) {
        for (spins = 1; !c.dead; ++spins) {
          const unsigned long long miss = __builtin_amdgcn_ballot_w64(umax3(umax3(mx4(pf[0]), mx4(pf[1]), mx4(pf[2])), mx4(pf[3]), mx4(pf[4])) == SENT);
          if (miss == 0ull) break;
          if (spins == 1 && __builtin_popcountll(miss) > 32) { *(LAS int*)(c.lds + B_FLAG + 4) = s + 1; wait_raw(SBt + c.pD + 25); }
          if (spins > 1) __builtin_amdgcn_s_sleep(3);
          pin_v(poff);
          MEMF();
#pragma unroll
          for (int f = 0; f < 5; ++f) pf[f] = ld16c(c.rw, poff + f * 128, slot + MB_2ND);
          if (spins > SPIN_CAP) give_up(c, 0x400u + s);
        }
        d = f32x4{0, 0, 0, 0};
#pragma unroll
        for (int f = 0; f < 5; ++f) d = mfma32(z[f], pf[f], d);
        *(LAS f32x4*)redw = d;
        *(LAS int*)(c.lds + B_FLAG) = s + 1;
      }
      bar();
      {
        u32x2 o = b_reduce_load(c.lds + B_RED, lane);
        o.x = nosent(o.x); o.y = nosent(o.y);
        st8c(c.rw, poff0, so1, o);
      }
      SB0();
      pD = cyc();
      if (master) {
        const unsigned tn = rtnow(), pw = __builtin_amdgcn_readfirstlane(par);
        const unsigned LA = (pw ? pw >> 16 : c.pLA) + (s == 0 ? c.pInc : 0), LB = (pw ? pw & 0xffffu : c.pLB) + (s < 2 ? 10 : 0);
        const int late = rtdiff(tn, SAn);
        const int ext = __builtin_amdgcn_readfirstlane(*(LAS int*)(c.lds + B_FLAG + 4)) == s + 1 ? 21 : 0;
        const unsigned base = late > c.pD + 14 + ext ? tn + c.pPen : (late > 2 ? SAn + c.pD + 24 + ext : SAn);
        const unsigned nSB = (base + LA) & 0x7fffffffu, nSA = (nSB + LB) & 0x7fffffffu;
        st8c(c.rw, ooff, so1, u32x2{nSB, nSA});
      }
      pW = twake; pS = SBt;
      pBad = __builtin_amdgcn_readfirstlane(*(LAS int*)(c.lds + B_FLAG)) == s + 1; pL = d0;
    }
    SB0();
    b_outx_mma(redw, zo, px);
    SB0();
    if constexpr (!LAST) {
      wait_rt(SAn + c.pDM);
#pragma unroll
      for (int sg = 0; sg < 3; ++sg) m3[sg] = ld16c(c.rw, l16, c.mb + ((s + 1) & 7) * (MB_SLOT * 2) + moff[sg]);
      SB0();
      if (wave == 2) repk = pace_report(pace, s, pS, pW, pD, pBad, pL);
      st4c(c.rw, rpw, c.mb, repk);
      st4c(c.rw, s > LAG + 1 ? fao : OOB, OFF_CTL, (unsigned)(s - LAG - 1));
    }
    st8c(c.rw, s > LAG ? aoff : OOB, xblk(c, s > LAG ? s - 1 - LAG : 0) + XB_AO + l2p(j * 512), ao);
    SB0();
    if constexpr (!LAST) {
      const int need = min(s + fa - ((s + fb) & fm), NSEG);
      if (__builtin_expect(__builtin_amdgcn_ballot_w64(fv < need) != 0ull, 0)) {
        for (unsigned spins = 0; !c.dead;) {
          MEMF();
          fv = (int)ld4c(c.rw, foff, OFF_CTL);
          if (__builtin_amdgcn_ballot_w64(fv < need) == 0ull) break;
          __builtin_amdgcn_s_sleep(2);
          if (++spins > SPIN_CAP) give_up(c, 0x500u + s);
        }
      }
      MEMF();
    }
    SA = SAn;
    rs = rs1;
  };
  for (int s = 0; s < NSEG - 1; ++s) step(s, BoolC<false>{});
  step(NSEG - 1, BoolC<true>{});
  MS(4);
  u32x4 pa[4][5], pb[4][5], pc[4][5];
  auto request = [&](int sx, u32x4 (&px)[4][5]) { wait_prog<1>(c, PG_PX + (sx & 1), 8 * wave, 7, sx + 1, 0); b_px_issue(c, sx, px); };
  auto finish = [&](int sx, const u32x4 (&px)[4][5]) {
    rs = rs + 1 == NRING ? 0 : rs + 1;
    u32x4 zo[5];
#pragma unroll
    for (int f = 0; f < 5; ++f) zo[f] = *(LAS u32x4*)(c.lds + B_RING + rs * 20480 + wave * 5120 + f * 1024 + lane * 16);
    bar();
    b_outx_mma(redw, zo, px);
    bar();
    b_outx_fin(c, j, sx, aoff);
  };
  static_assert(LAG == 4, "tail");
  bar();
  b_outx_fin(c, j, 59, aoff);
  if (__builtin_amdgcn_ballot_w64(fcl != 0 && fv < 60 + fcl) == 0ull) { b_px_issue(c, 60, pa); b_px_issue(c, 61, pb); }
  else { request(60, pa); request(61, pb); }
  waitvm<28>();
  bar();
  st4c(c.rw, fao, OFF_CTL, 60u);
  finish(60, pa);
  request(62, pc);
  finish(61, pb);
  st4c(c.rw, fao, OFF_CTL, 61u);
  request(63, pb);
  finish(62, pc);
  st4c(c.rw, fao, OFF_CTL, 62u);
  finish(63, pb);
  bar_vm();
  st4c(c.rw, fao, OFF_CTL, (unsigned)NSEG);
}

constexpr int G_HBUF = 0;
template <class F> DEV void g_load_w(Ctx& c, u32x4 (&W)[20][3], const u16* Wm, int ldb, int col0, F meanwhile) {
  const rsrc_t rW = mkrsrc(Wm, DM * ldb);
  int voff[6], ta[3];
  blk_voff(c.lane, c.wave, ldb, false, voff); blk_toff(c.lane, c.wave, ta);
  auto issue = [&](int b) { dma_grp<6, (NTM & 2) != 0>(rW, voff, b * 64 * ldb + col0 * 2, c.ldsb + (b % 6) * BLK + c.wave * 6144); };
#pragma unroll
  for (int b = 0; b < 6; ++b) issue(b);
  meanwhile();
#pragma unroll
  for (int b = 0; b < 10; ++b) {
    if (b == 0) waitvm<30>(); else if (b < 6) waitvm<24>(); else if (b == 6) waitvm<18>(); else if (b == 7) waitvm<12>(); else if (b == 8) waitvm<6>(); else waitvm<0>();
    bar();
    if (b >= 1 && b + 5 < 10) issue(b + 5);
#pragma unroll
    for (int hf = 0; hf < 2; ++hf)
#pragma unroll
      for (int t = 0; t < 3; ++t) W[2 * b + hf][t] = blk_frag(c.lds + (b % 6) * BLK + ta[t], hf);
#pragma unroll
    for (int hf = 0; hf < 2; ++hf)
#pragma unroll
      for (int t = 0; t < 3; ++t) { if (b < 9) pin_a(W[2 * b + hf][t]); else pin_v(W[2 * b + hf][t]); }
  }
}
DEV void flag_store(Ctx& c, int kind, int h, unsigned val) {
  if (c.wave == 0 && c.lane == 0) st4c(c.rw, pgoff(c, kind, h), OFF_CTL, val);
}
template <bool FETCH> DEV void g_gemm(Ctx& c, const u32x4 (&W)[20][3], LAS unsigned char* hb, f32x4 (&acc)[2][3], int so, unsigned lo) {
  constexpr int PF = 3;
  const int voff = c.lane * 16;
  u32x4 h0[20], h1[20];
  const f32x4 z4 = {0, 0, 0, 0};
#pragma unroll
  for (int ks = 0; ks < PF; ++ks) { h0[ks] = *(LAS u32x4*)(hb + ks * 1024); h1[ks] = *(LAS u32x4*)(hb + (20 + ks) * 1024); }
#pragma unroll
  for (int ks = 0; ks < 20; ++ks) {
    SB0();
    acc[0][0] = mfma32(W[ks][0], h0[ks], ks ? acc[0][0] : z4);
    SB0();
    if (ks + PF < 20) h0[ks + PF] = *(LAS u32x4*)(hb + (ks + PF) * 1024);
    SB0();
    acc[1][0] = mfma32(W[ks][0], h1[ks], ks ? acc[1][0] : z4);
    SB0();
    if (ks + PF < 20) h1[ks + PF] = *(LAS u32x4*)(hb + (20 + ks + PF) * 1024);
    SB0();
    acc[0][1] = mfma32(W[ks][1], h0[ks], ks ? acc[0][1] : z4);
    SB0();
    if (FETCH && ks < 10 && (ks & 3) == 0) dma_m0(lo + ks * 1024);
    SB0();
    acc[1][1] = mfma32(W[ks][1], h1[ks], ks ? acc[1][1] : z4);
    SB0();
    if (FETCH && ks < 10) {
      if ((ks & 3) == 0) dma_off<0>(c.rw, voff, so + (ks & ~3) * 1024);
      if ((ks & 3) == 1) dma_off<1024>(c.rw, voff, so + (ks & ~3) * 1024);
      if ((ks & 3) == 2) dma_off<2048>(c.rw, voff, so + (ks & ~3) * 1024);
      if ((ks & 3) == 3) dma_off<3072>(c.rw, voff, so + (ks & ~3) * 1024);
    }
    SB0();
    acc[0][2] = mfma32(W[ks][2], h0[ks], ks ? acc[0][2] : z4);
    acc[1][2] = mfma32(W[ks][2], h1[ks], ks ? acc[1][2] : z4);
  }
  SB0();
}
DEV void role_G(Ctx& c, int h, int kind, const u16* Wm, const u16* x, const u16* mem0, GAS unsigned* ctl) {
  const int lane = c.lane, wave = c.wave;
  u32x4 W[20][3];
  u32x4 xr[4][3];
  ln1_issue(c, xr, kind ? 2 * h + kind - 1 : -1, x);
  g_load_w(c, W, Wm, LDW * 2, h * DH, [&]() {
    waitvm<36>();
    if (kind) {
      ln1_norm(c, xr);
      for (int q = 0; q < 3; ++q) mbx_init(c, 64 * q + 2 * h + kind - 1, mem0, c.mb);
      ln1_seg(c, xr, 2 * h + kind - 1);
      count_up(c, ctl, CT_LN);
      MS(3);
    }
  });
  MS(1);
  if (wave == 0) { wait_count(c, CT_LN, 64); __builtin_amdgcn_fence(__ATOMIC_ACQUIRE, "agent"); }
  bar_vm();
  {
    const int hv[10] = {lane * 16, lane * 16, lane * 16, lane * 16, lane * 16 + 4096, lane * 16 + 4096, lane * 16 + 4096, lane * 16 + 4096, lane * 16 + 8192, lane * 16 + 8192};
    dma_grp<10, false>(c.rw, hv, OFF_HF + wave * 10240, c.ldsb + G_HBUF + wave * 10240);
  }
  bar_vm();
  MS(2);
  const int obase = (kind == 0 ? OFF_QX : (kind == 1 ? OFF_KX : OFF_VX)) + (h * 4 + wave) * 3072;
  const int voff = lane * 16;
  for (int s = 0; s < NSEG; ++s) {
    const int sn = s + 1 < NSEG ? s + 1 : s - 1;
    f32x4 acc[2][3];
    g_gemm<true>(c, W, c.lds + G_HBUF + (s & 1) * 40960 + lane * 16, acc, OFF_HF + sn * 40960 + wave * 10240, c.ldsb + G_HBUF + (sn & 1) * 40960 + wave * 10240);
    const int ob = obase + s * (NH * 4 * 3072);
    st16c(c.rw, voff, ob, cat(pk4(acc[0][0]), pk4(acc[0][1])));
    st16c(c.rw, voff + 1024, ob, cat(pk4(acc[1][0]), pk4(acc[1][1])));
    st16c(c.rw, voff + 2048, ob, cat(pk4(acc[0][2]), pk4(acc[1][2])));
    if (s < 2) waitvm<0>(); else waitvm<3>();
    bar();
    flag_store(c, kind, h, s < 2 ? s + 1 : s);
    if (s == THR) {
#pragma unroll
      for (int q = 0; q < 4; ++q) wait_count(c, CT_WVO + 32 * q, NH);
    }
  }
  publish_prog(c, kind, h, NSEG);
  MS(4);
}

DEV void role_N(Ctx& c, int ty, int i, const u16* x, unsigned char* ws) {
  const int lane = c.lane, wave = c.wave, l32 = lane & 31;
  const bool has3 = l32 < 16;
  const rsrc_t rx = mkrsrc(x, NTOK * DM * 2);
  float ga[24], sh[24], scl[24];
  wait_count(c, CT_MOD2, 40);
  mbx_ready(c);
  load_mod(c, ga, 2); load_mod(c, sh, 3); load_mod(c, scl, 4);
  const int sl = wave * 2 + (lane >> 5), tk = tokslot(i & 1, (i & 2) * 4 + sl);
  const int f0 = pgoff(c, PG_AO, lane), f1 = pgoff(c, PG_AO, 64 + (lane & 15));
  for (int s = ty + 4 * (i >> 2); s < NSEG; s += 8) {
    const int T = s * 32 + tk;
    u32x4 w[3];
#pragma unroll
    for (int u = 0; u < 3; ++u) w[u] = ld16n(rx, (u < 2 || has3) ? T * (DM * 2) + (l32 + 32 * u) * 16 : OOB, 0);
    if (s < NSEG - 4) watch<6>(c, PG_AO, ty * 8 + i, s + 1); else watch<6>(c, PG_AO, ty * 8 + i, NSEG - 6);
    for (unsigned spins = 0; !c.dead;) {
      MEMF();
      const unsigned m = min(ld4c(c.rw, f0, OFF_CTL), ld4c(c.rw, f1, OFF_CTL));
      if (__builtin_amdgcn_ballot_w64(m <= (unsigned)s) == 0ull) break;
      __builtin_amdgcn_s_sleep(2);
      if (++spins > SPIN_CAP) give_up(c, 0x700u + s);
    }
    MEMF();
    u32x4 av[3];
#pragma unroll
    for (int u = 0; u < 3; ++u) av[u] = ld16c(c.rw, (u < 2 || has3) ? l2p(((l32 + 32 * u) * 32 + tk) * 16) : OOB, xblk(c, s) + XB_AO);
    float v[24];
#pragma unroll
    for (int u = 0; u < 3; ++u) {
      const u32x4 a = av[u];
#pragma unroll
      for (int k = 0; k < 4; ++k) {
        float e0 = ga[u * 8 + 2 * k] * lo2f(a[k]), e1 = ga[u * 8 + 2 * k + 1] * hi2f(a[k]); rbf2(e0, e1);
        e0 += lo2f(w[u][k]); e1 += hi2f(w[u][k]); rbf2(e0, e1);
        v[u * 8 + 2 * k] = e0; v[u * 8 + 2 * k + 1] = e1;
      }
      st16c(c.rw, (u < 2 || has3) ? T * (DM * 2) + (l32 + 32 * u) * 16 : OOB, OFF_X1, pk8(v + u * 8));
    }
    ln_row(v, has3);
    modulate(v, sh, scl);
    ln_row(v, has3);
#pragma unroll
    for (int u = 0; u < 3; ++u) if (u < 2 || has3) *(LAS u32x4*)(c.lds + (l32 + 32 * u) * 144 + sl * 16) = pk8(v + u * 8);
    bar();
#pragma unroll
    for (int k = 0; k < 3; ++k) {
      const int p = (wave * 3 + k) * 8 + (lane >> 3);
      st16c(c.rw, p < 80 ? p * 256 + (lane & 7) * 16 : OOB, OFF_YF + (s * 2 + (i & 1)) * 20480 + (i & 2) * 64, *(LAS u32x4*)(c.lds + p * 144 + (lane & 7) * 16));
    }
    publish_prog(c, PG_N, ty * 8 + i, s + 1);
  }
}
DEV void wait_n(Ctx& c, int ty, int s) {
  if (s < NSEG - 4) watch<4>(c, PG_N, ty * 8 + ((s >> 2) & 1) * 4, s + 1);
  const int fo = pgoff(c, PG_N, ty * 8 + ((s >> 2) & 1) * 4 + (c.lane & 3));
  for (unsigned spins = 0; !c.dead;) {
    MEMF();
    if (__builtin_amdgcn_ballot_w64(ld4c(c.rw, fo, OFF_CTL) <= (unsigned)s) == 0ull) break;
    __builtin_amdgcn_s_sleep(1);
    if (++spins > SPIN_CAP) give_up(c, 0x800u + s);
  }
  MEMF();
}
DEV void role_M1(Ctx& c, int ty, int i, const u16* w1, const u16* b1) {
  const int lane = c.lane, wave = c.wave, g = lane >> 4;
  u32x4 W[20][3];
  g_load_w(c, W, w1, FF * 2, i * DH, []() {});
  float bia[12];
#pragma unroll
  for (int t = 0; t < 3; ++t) { const u32x2 b = *(const u32x2*)(b1 + i * DH + 48 * wave + 16 * t + 4 * g); bia[4 * t] = lo2f(b.x); bia[4 * t + 1] = hi2f(b.x); bia[4 * t + 2] = lo2f(b.y); bia[4 * t + 3] = hi2f(b.y); }
  bar();
  MS(5);
  for (int s = ty; s < NSEG; s += 4) {
    wait_n(c, ty, s);
    u32x4 yv[10];
#pragma unroll
    for (int k = 0; k < 10; ++k) yv[k] = ld16c(c.rw, lane * 16 + (k & 3) * 1024, OFF_YF + s * 40960 + wave * 10240 + (k & ~3) * 1024);
#pragma unroll
    for (int k = 0; k < 10; ++k) *(LAS u32x4*)(c.lds + G_HBUF + (wave * 10 + k) * 1024 + lane * 16) = yv[k];
    bar();
    f32x4 acc[2][3];
    g_gemm<false>(c, W, c.lds + G_HBUF + lane * 16, acc, 0, 0u);
    u32x2 o[2][3];
#pragma unroll
    for (int tt = 0; tt < 2; ++tt)
#pragma unroll
      for (int t = 0; t < 3; ++t)
#pragma unroll
        for (int r = 0; r < 4; r += 2) {
          const unsigned r1 = pk2(acc[tt][t][r], acc[tt][t][r + 1]);
          const unsigned r2 = pk2(lo2f(r1) + bia[4 * t + r], hi2f(r1) + bia[4 * t + r + 1]);
          o[tt][t][r >> 1] = pk2(gelu_t(lo2f(r2)), gelu_t(hi2f(r2)));
        }
    const int ob = OFF_HID + ((s * 8 + i) * 4 + wave) * 3072;
    st16c(c.rw, lane * 16, ob, cat(o[0][0], o[0][1]));
    st16c(c.rw, lane * 16 + 1024, ob, cat(o[1][0], o[1][1]));
    st16c(c.rw, lane * 16 + 2048, ob, cat(o[0][2], o[1][2]));
    publish_prog(c, PG_M1, ty * 8 + i, s + 1);
  }
}

DEV void p_frags(const u32x4 (&f)[12], int tt, u32x4 (&a)[6]) {
#pragma unroll
  for (int w = 0; w < 4; ++w) a[w] = f[w * 3 + tt];
#pragma unroll
  for (int k = 0; k < 2; ++k)
    a[4 + k] = tt ? u32x4{f[6 * k + 2].z, f[6 * k + 2].w, f[6 * k + 5].z, f[6 * k + 5].w} : u32x4{f[6 * k + 2].x, f[6 * k + 2].y, f[6 * k + 5].x, f[6 * k + 5].y};
}
DEV float xmax4(float v) {
  auto r = __builtin_amdgcn_permlane16_swap(fbits(v), fbits(v), false, false);
  v = fmaxf(bitsf(r[0]), bitsf(r[1]));
  auto q = __builtin_amdgcn_permlane32_swap(fbits(v), fbits(v), false, false);
  return fmaxf(bitsf(q[0]), bitsf(q[1]));
}
DEV float xsum4(float v) {
  auto r = __builtin_amdgcn_permlane16_swap(fbits(v), fbits(v), false, false);
  v = bitsf(r[0]) + bitsf(r[1]);
  auto q = __builtin_amdgcn_permlane32_swap(fbits(v), fbits(v), false, false);
  return bitsf(q[0]) + bitsf(q[1]);
}
DEV void role_P(Ctx& c, int pi) {
  const int lane = c.lane, wave = c.wave, g = lane >> 4, n = lane & 15;
  const int h = 2 * pi + (wave >> 1), par = wave & 1;
  mbx_ready(c);
  const int kmoff = n >= 8 ? (g * 8 + n - 8) * 8 : OOB / 2;
  const int fpx = lane == 0 ? pgoff(c, (PG_PX + par), h) : OOB;
  unsigned haveQ = 0, haveK = 0, haveKM = 0;
  const bool conductor = pi == 0 && wave == 0;
  int la = c.pLA - c.pMA, lb = c.pLB - c.pMB, ma1 = 0, mb1 = 0, xm = 4, calm = 0;
  for (int s = par; s < NSEG; s += 2) {
    haveQ = wait_prog(c, PG_Q, h, 0, s + 1, haveQ);
    haveK = wait_prog(c, PG_K, h, 0, s + 1, haveK);
    haveKM = wait_prog<4>(c, PG_KM, h, 0, s, haveKM);
    u32x4 qf[12], kf[12];
#pragma unroll
    for (int i = 0; i < 12; ++i) qf[i] = ld16c(c.rw, lane * 16 + (i & 3) * 1024, OFF_QX + (s * NH + h) * 12288 + (i >> 2) * 4096);
#pragma unroll
    for (int i = 0; i < 12; ++i) kf[i] = ld16c(c.rw, lane * 16 + (i & 3) * 1024, OFF_KX + (s * NH + h) * 12288 + (i >> 2) * 4096);
    SB0();
    u32x4 q[2][6];
    p_frags(qf, 0, q[0]); p_frags(qf, 1, q[1]);
    float v[2][12], mx[2];
    {
      u32x4 a[2][6];
      p_frags(kf, 0, a[0]); p_frags(kf, 1, a[1]);
#pragma unroll
      for (int tq = 0; tq < 2; ++tq) {
        const int tk = tokslot(tq, n);
        f32x4 sx[2];
#pragma unroll
        for (int tt = 0; tt < 2; ++tt) {
          sx[tt] = f32x4{0, 0, 0, 0};
#pragma unroll
          for (int ks = 0; ks < 6; ++ks) sx[tt] = mfma32(a[tt][ks], q[tq][ks], sx[tt]);
        }
        mx[tq] = -INFINITY;
#pragma unroll
        for (int k = 0; k < 8; k += 2) {
          const unsigned r1 = pk2(sx[k >> 2][k & 3], sx[k >> 2][(k & 3) + 1]), r2 = pk2(lo2f(r1) * SCALE, hi2f(r1) * SCALE);
          v[tq][k] = 8 * g + k <= tk ? lo2f(r2) : -INFINITY; v[tq][k + 1] = 8 * g + k + 1 <= tk ? hi2f(r2) : -INFINITY;
          mx[tq] = fmaxf(mx[tq], fmaxf(v[tq][k], v[tq][k + 1]));
        }
#pragma unroll
        for (int k = 0; k < 8; ++k) pin_v(v[tq][k]);
        pin_v(mx[tq]);
      }
    }
    if (conductor && s > 3) {
      const u32x4 r0 = ld16c(c.rw, l2p(MB_RPT + lane * 128), c.mb);
      const unsigned r1 = ld4c(c.rw, lane < 48 ? l2p(MB_RPT + (64 + lane) * 128) : OOB, c.mb);
      auto cl = [](unsigned w) { return (w & 0x4000u) ? 0u : (w & 0x3fffu); };
      const int ma = (int)wave_umax(lane < 32 ? max(max(cl(r0.x), cl(r0.y)), max(cl(r0.z), cl(r0.w))) : 0u);
      const int mb = (int)wave_umax(max(lane >= 32 ? cl(r0.x) : 0u, cl(r1)));
      const bool bad = __builtin_amdgcn_ballot_w64(((r0.x | r0.y | r0.z | r0.w | r1) & 0x4000u) != 0u) != 0ull;
      if (!bad) {
        la = ma > la ? min(ma, la + 4) : max(max(ma, ma1), la - (s < 10 ? 6 : 1)); ma1 = ma;
        lb = mb > lb ? min(mb, lb + 4) : max(max(mb, mb1), lb - (s < 10 ? 6 : 1)); mb1 = mb;
        if (++calm >= (s < 20 ? 2 : ADEC)) { calm = 0; xm = max(xm - 1, 0); }
      } else {
        if (s > 6 && (calm >= 0 || calm < -2)) xm = min(xm + AINC, 24);
        calm = calm >= 0 ? -1 : calm - 1;
      }
      if (!bad && calm < 0) calm = 0;
      st4c(c.rw, lane == 0 ? l2p(MB_RPT + 127 * 128) : OOB, c.mb, ((unsigned)(la + c.pMA + xm) << 16) | (unsigned)(lb + c.pMB + xm));
    }
    haveKM = wait_prog<4>(c, PG_KM, h, 0, s + 1, haveKM);
    u32x4 km[6];
    u32x2 kb[4];
#pragma unroll
    for (int w = 0; w < 4; ++w) {
      km[w] = ld16c(c.rw, kmoff * 2, xblk(c, s) + km_piece(h * 4 + w));
      kb[w] = ld8c(c.rw, kmoff + 512, xblk(c, s) + km_piece(h * 4 + w));
    }
    SB0();
    km[4] = cat(kb[0], kb[1]); km[5] = cat(kb[2], kb[3]);
    const int ob = xblk(c, s) + p_head(h);
#pragma unroll
    for (int tq = 0; tq < 2; ++tq) {
      const int tk = tokslot(tq, n);
      f32x4 sm = {0, 0, 0, 0};
#pragma unroll
      for (int ks = 0; ks < 6; ++ks) sm = mfma32(km[ks], q[tq][ks], sm);
#pragma unroll
      for (int k = 0; k < 4; k += 2) {
        const unsigned r1 = pk2(sm[k], sm[k + 1]), r2 = pk2(lo2f(r1) * SCALE, hi2f(r1) * SCALE);
        v[tq][8 + k] = g >= 2 ? lo2f(r2) : -INFINITY; v[tq][9 + k] = g >= 2 ? hi2f(r2) : -INFINITY;
        mx[tq] = fmaxf(mx[tq], fmaxf(v[tq][8 + k], v[tq][9 + k]));
      }
      const float ml = xmax4(mx[tq]) * LOG2E;
      float sum = 0.f;
#pragma unroll
      for (int k = 0; k < 12; ++k) { v[tq][k] = __builtin_amdgcn_exp2f(__builtin_fmaf(v[tq][k], LOG2E, -ml)); sum += v[tq][k]; }
      const float inv = __builtin_amdgcn_rcpf(xsum4(sum));
#pragma unroll
      for (int k = 0; k < 12; ++k) { v[tq][k] *= inv; pin_v(v[tq][k]); }
    }
#pragma unroll
    for (int tq = 0; tq < 2; ++tq) {
      const int tk = tokslot(tq, n);
      st16c(c.rw, g <= (tk >> 3) ? (tk >> 3) * 49152 + (1 + g) * 128 + (tk & 7) * 16 : OOB, ob, pk8(v[tq]));
      st8c(c.rw, g >= 2 ? (tk >> 3) * 49152 + (tk & 7) * 16 + (g & 1) * 8 : OOB, ob, u32x2{pk2(v[tq][8], v[tq][9]), pk2(v[tq][10], v[tq][11])});
    }
    waitvm<0>();
    st4c(c.rw, fpx, OFF_CTL, (unsigned)(s + 1));
  }
}

DEV void role_M2(Ctx& c, int ty, int i, const u16* w2, const u16* b2, const unsigned char* ws, u16* out) {
  const int lane = c.lane, wave = c.wave, g = lane >> 4, n = lane & 15;
  u32x4 W[12][5];
  {
    const rsrc_t rW = mkrsrc(w2, FF * DM * 2);
    int va[5], vb[5];
#pragma unroll
    for (int k = 0; k < 5; ++k) {
      const int p = k * 64 + lane, r = p / 10, ch = (p % 10) * 16 - (k & 3) * 1024;
      va[k] = r * (DM * 2) + ch; vb[k] = (48 * (r >> 4) + 32 + (r & 15)) * (DM * 2) + ch;
    }
    auto issue = [&](int u) {
      const int ks = u % 6, so = ((2 * wave + u / 6) * DH + (ks < 4 ? 48 * ks : 96 * (ks - 4))) * (DM * 2) + i * 160;
      if (ks < 4) dma_grp<5>(rW, va, so, c.ldsb + wave * 30720 + ks * 5120); else dma_grp<5>(rW, vb, so, c.ldsb + wave * 30720 + ks * 5120);
    };
    waitvm<0>();
#pragma unroll
    for (int u = 0; u < 6; ++u) issue(u);
#pragma unroll
    for (int u = 0; u < 12; ++u) {
      if (u < 7) waitvm<25>(); else if (u == 7) waitvm<20>(); else if (u == 8) waitvm<15>(); else if (u == 9) waitvm<10>(); else if (u == 10) waitvm<5>(); else waitvm<0>();
#pragma unroll
      for (int t = 0; t < 5; ++t) {
        LAS unsigned char* a = c.lds + wave * 30720 + (u % 6) * 5120 + (4 * g + (n >> 2)) * 160 + t * 32 + (lane & 3) * 8;
        W[u][t] = cat(trread(a), trread(a + 16 * 160));
      }
#pragma unroll
      for (int t = 0; t < 5; ++t) { if (u < 10) pin_a(W[u][t]); else pin_v(W[u][t]); }
      if (u < 6) issue(u + 6);
    }
  }
  const int tt = wave & 1, t0 = (wave >> 1) * 3;
  wait_count(c, CT_MOD2, 40);
  const rsrc_t ro = mkrsrc(out, NTOK * DM * 2);
  u32x2 bv[3]; f32x4 gb[3];
#pragma unroll
  for (int k = 0; k < 3; ++k) { const int col = i * 80 + min(t0 + k, 4) * 16 + 4 * g; bv[k] = *(const u32x2*)(b2 + col); gb[k] = __builtin_bit_cast(f32x4, ld16c(c.rw, col * 4, OFF_MOD + 5 * DM * 4)); }
  bar();
  MS(5);
  const int fo = pgoff(c, PG_M1, ty * 8 + 2 * wave + (lane & 1));
  for (int s = ty; s < NSEG; s += 4) {
    wait_n(c, ty, s);
    const int xo = (s * 32 + tokslot(tt, n)) * (DM * 2) + (i * 80 + t0 * 16 + 4 * g) * 2;
    u32x2 xv[3];
#pragma unroll
    for (int k = 0; k < 3; ++k) xv[k] = ld8c(c.rw, t0 + k < 5 ? xo + k * 32 : OOB, OFF_X1);
    for (unsigned spins = 0; !c.dead;) {
      MEMF();
      if (__builtin_amdgcn_ballot_w64(ld4c(c.rw, fo, OFF_CTL) <= (unsigned)s) == 0ull) break;
      __builtin_amdgcn_s_sleep(1);
      if (++spins > SPIN_CAP) give_up(c, 0x900u + s);
    }
    MEMF();
    u32x4 hf[2][12];
#pragma unroll
    for (int half = 0; half < 2; ++half)
#pragma unroll
      for (int k = 0; k < 12; ++k) hf[half][k] = ld16c(c.rw, lane * 16 + (k & 3) * 1024, OFF_HID + (s * 8 + 2 * wave + half) * 12288 + (k >> 2) * 4096);
    SB0();
    f32x4 acc[2][5];
#pragma unroll
    for (int t = 0; t < 5; ++t) { acc[0][t] = f32x4{0, 0, 0, 0}; acc[1][t] = f32x4{0, 0, 0, 0}; }
#pragma unroll
    for (int half = 0; half < 2; ++half) {
      u32x4 a[2][6];
      p_frags(hf[half], 0, a[0]); p_frags(hf[half], 1, a[1]);
#pragma unroll
      for (int ks = 0; ks < 6; ++ks)
#pragma unroll
        for (int t = 0; t < 5; ++t) { acc[0][t] = mfma32(W[half * 6 + ks][t], a[0][ks], acc[0][t]); acc[1][t] = mfma32(W[half * 6 + ks][t], a[1][ks], acc[1][t]); }
    }
#pragma unroll
    for (int m = 0; m < 2; ++m)
#pragma unroll
      for (int t = 0; t < 5; ++t) *(LAS f32x4*)(c.lds + ((wave * 10 + m * 5 + t) * 64 + lane) * 16) = acc[m][t];
    bar();
#pragma unroll
    for (int k = 0; k < 3; ++k) {
      LAS unsigned char* rp = c.lds + ((tt * 5 + min(t0 + k, 4)) * 64 + lane) * 16;
      const f32x4 sm = (*(LAS f32x4*)rp + *(LAS f32x4*)(rp + 10240)) + (*(LAS f32x4*)(rp + 20480) + *(LAS f32x4*)(rp + 30720));
      float o0 = sm[0], o1 = sm[1], o2 = sm[2], o3 = sm[3];
      rbf2(o0, o1); rbf2(o2, o3);
      o0 += lo2f(bv[k].x); o1 += hi2f(bv[k].x); o2 += lo2f(bv[k].y); o3 += hi2f(bv[k].y);
      rbf2(o0, o1); rbf2(o2, o3);
      o0 *= gb[k][0]; o1 *= gb[k][1]; o2 *= gb[k][2]; o3 *= gb[k][3];
      rbf2(o0, o1); rbf2(o2, o3);
      st8(ro, t0 + k < 5 ? xo + k * 32 : OOB, 0, u32x2{pk2(o0 + lo2f(xv[k].x), o1 + hi2f(xv[k].x)), pk2(o2 + lo2f(xv[k].y), o3 + hi2f(xv[k].y))});
    }
    bar();
  }
}

DEV const u16* karg(int i);
DEV void role_O(Ctx& c, int h, const u16* Wo, GAS unsigned* ctl) {
  const int lane = c.lane, wave = c.wave, g = lane >> 4, n = lane & 15;
  u32x4 W[6][10];
  {
    const rsrc_t rW = mkrsrc(Wo, LDW * DM * 2);
    int va[10], vb[10];
#pragma unroll
    for (int i = 0; i < 10; ++i) {
      const int p = i * 64 + lane, r = p / 20, ch = (p % 20) * 16 - (i & 3) * 1024;
      va[i] = r * (DM * 2) + ch; vb[i] = (48 * (r >> 4) + 32 + (r & 15)) * (DM * 2) + ch;
    }
    const int colb = h * DH * DM * 2 + wave * 320;
    const unsigned ldsw = c.ldsb + wave * 30720;
    waitvm<0>();
#pragma unroll
    for (int ks = 0; ks < 3; ++ks) dma_grp<10, (NTM & 8) != 0>(rW, va, colb + 48 * ks * (DM * 2), ldsw + ks * 10240);
#pragma unroll
    for (int ks = 0; ks < 6; ++ks) {
      if (ks < 4) waitvm<20>(); else if (ks == 4) waitvm<10>(); else waitvm<0>();
#pragma unroll
      for (int t = 0; t < 10; ++t) {
        LAS unsigned char* a = c.lds + wave * 30720 + (ks % 3) * 10240 + (4 * g + (n >> 2)) * 320 + t * 32 + (lane & 3) * 8;
        W[ks][t] = cat(trread(a), trread(a + 16 * 320));
      }
#pragma unroll
      for (int t = 0; t < 10; ++t) { if (t < 9) pin_a(W[ks][t]); else pin_v(W[ks][t]); }
      if (ks == 0) dma_grp<10, (NTM & 8) != 0>(rW, va, colb + 48 * 3 * (DM * 2), ldsw);
      if (ks == 1 || ks == 2) dma_grp<10, (NTM & 8) != 0>(rW, vb, colb + 96 * (ks - 1) * (DM * 2), ldsw + ks * 10240);
    }
  }
  const int ov = (n >> 3) * (NH * 512) + g * 128 + (n & 7) * 16;
  MS(1);
  bar();
  if (h < 28) mod_part(c, 32 + h, karg(2), karg(3), ctl);
  unsigned haveV = wait_prog(c, PG_V, h, 0, 1, 0), haveKM = 0;
  MS(2);
  u32x4 va[12], vb[12];
#pragma unroll
  for (int i = 0; i < 12; ++i) va[i] = ld16c(c.rw, lane * 16 + (i & 3) * 1024, OFF_VX + h * 12288 + (i >> 2) * 4096);
  int fo = (lane & 1) ? OOB : pgoff(c, PG_V, h);
  int roff = OOB;
  if (lane < 40) {
    const int y = (h * 2 + (wave & 1)) * 640 + lane * 16, cp = y / 10240;
    roff = wave < 2 ? p_head(h) + lane * 16 + wave * MB_2ND : l2p((cp == 0 ? MB_MA : (cp == 1 ? MB_M2 : (cp == 2 ? MB_MB : MB_M3))) + y % 10240);
  } else if (lane < 42 && h == 0 && wave == 0) roff = l2p(lane == 40 ? MB_HA : MB_HB);
  auto seg = [&](int s, const u32x4 (&vf)[12], u32x4 (&vn)[12]) __attribute__((always_inline)) {
    u32x4 a[2][6];
    p_frags(vf, 0, a[0]); p_frags(vf, 1, a[1]);
    const bool pre = s + 1 < NSEG && haveV >= (unsigned)(s + 2);
    int vv = pre ? lane * 16 : OOB;
    const int vo = OFF_VX + ((pre ? s + 1 : s) * NH + h) * 12288;
    if (s == 6) { mbx_ready(c); fo = pgoff(c, (lane & 1) ? PG_KM : PG_V, h); }
    if (s >= UP) haveKM = wait_prog<16>(c, PG_KM, h, 0, s - UP + 1, haveKM);
    st16c(c.rw, s >= 7 ? roff : OOB, c.mb + ((s + 1) & 7) * (MB_SLOT * 2), u32x4{SENT, SENT, SENT, SENT});
    const int ub = OFF_U + (((s & UR) * NBC + 20 * wave) * NH + h) * 512;
    unsigned fl;
    const f32x4 z4 = {0, 0, 0, 0};
    f32x4 a0[10], a1[10];
    u32x4 o;
    pin_v(vv);
#pragma unroll
    for (int t = 0; t <= 10; ++t) {
#pragma unroll
      for (int ks = 0; ks < 6; ++ks) {
        SB0();
        if (t < 10) { a0[t] = mfma32(a[0][ks], W[ks][t], ks ? a0[t] : z4); }
        SB0();
        if (t > 1 && ks == 0) st16c(c.rw, ov, ub + (t - 2) * (2 * NH * 512), o);
        if (t < 6 && (ks == 1 || ks == 3)) vn[2 * t + (ks >> 1)] = ld16c(c.rw, vv + ((2 * t + (ks >> 1)) & 3) * 1024, vo + ((2 * t + (ks >> 1)) >> 2) * 4096);
        if (t > 0 && ks == 4) o.x = pk2(a0[t - 1][0], a0[t - 1][1]);
        if (t > 0 && ks == 5) o.z = pk2(a1[t - 1][0], a1[t - 1][1]);
        SB0();
        if (t < 10) { a1[t] = mfma32(a[1][ks], W[ks][t], ks ? a1[t] : z4); }
        SB0();
        if (t == 6 && ks == 1) fl = ld4c(c.rw, fo, OFF_CTL);
        if (t > 0 && ks == 4) o.y = pk2(a0[t - 1][2], a0[t - 1][3]);
        if (t > 0 && ks == 5) o.w = pk2(a1[t - 1][2], a1[t - 1][3]);
      }
      if (t == 10) st16c(c.rw, ov, ub + 9 * (2 * NH * 512), o);
      if (t == 4 && s > 2) { waitvm<14>(); bar(); flag_store(c, PG_U, h, s); }
    }
    if (s < 2 || s + 1 == NSEG) publish_prog(c, PG_U, h, s + 1);
    haveV = __builtin_amdgcn_readlane(fl, 0); haveKM = __builtin_amdgcn_readlane(fl, 1);
    MEMF();
    if (s + 1 < NSEG && !pre) {
      haveV = wait_prog(c, PG_V, h, 0, s + 2, haveV);
#pragma unroll
      for (int i = 0; i < 12; ++i) vn[i] = ld16c(c.rw, lane * 16 + (i & 3) * 1024, OFF_VX + ((s + 1) * NH + h) * 12288 + (i >> 2) * 4096);
    }
  };
  for (int s = 0; s < NSEG; s += 2) { seg(s, va, vb); seg(s + 1, vb, va); }
}

struct KArgs { const u16* p[13]; u16* out; unsigned char* ws; int par[8]; };
static_assert(sizeof(KArgs) == 152, "no padding");
enum { A_X, A_MEM0, A_C, A_WMOD, A_WQ, A_WK, A_WV, A_WO, A_W1, A_B1, A_W2, A_B2, A_OUT = 13 };
DEV const u16* karg(int i) {
  auto q = (const __attribute__((address_space(4))) unsigned long long*)__builtin_amdgcn_kernarg_segment_ptr();
  asm volatile("" : "+s"(q));
  return (const u16*)q[i];
}
__global__ void __launch_bounds__(256, 1) k_rec(KArgs ka) {
  unsigned char* ws = ka.ws;
  extern __shared__ __attribute__((aligned(16))) unsigned char dyn[];
  Ctx c;
  c.lds = (LAS unsigned char*)dyn; c.ldsb = (unsigned)(size_t)c.lds;
  c.lane = threadIdx.x & 63; c.wave = __builtin_amdgcn_readfirstlane(threadIdx.x >> 6);
  c.rw = mkrsrc(ws, WS_USED); c.dead = false;
  c.pLA = ka.par[0]; c.pLB = ka.par[1]; c.pDM = ka.par[2]; c.pPen = ka.par[3]; c.pMA = ka.par[4]; c.pMB = ka.par[5]; c.pInc = ka.par[6]; c.pD = ka.par[7];
  GAS unsigned* ctl = (GAS unsigned*)(ws + OFF_CTL);
  if (threadIdx.x == 0) {
    const unsigned x = xcc_id();
    const unsigned t = __hip_atomic_fetch_add(ctl + CT_TICKET + 64 * x, 1u, RLX_AGENT);
    int role = -1;
    if (t < 32u) role = x * 32 + t;
    else {
      unsigned k = __hip_atomic_fetch_add(ctl + CT_SPARE, 1u, RLX_AGENT);
      unsigned cnt[8];
      for (unsigned spins = 0; spins < SPIN_CAP; ++spins) {
        unsigned tot = 0;
#pragma unroll
        for (int i = 0; i < 8; ++i) { cnt[i] = __hip_atomic_load(ctl + CT_TICKET + 64 * i, RLX_AGENT); tot += cnt[i]; }
        if (tot >= gridDim.x) break;
        __builtin_amdgcn_s_sleep(4);
      }
#pragma unroll
      for (int i = 0; i < 8; ++i) {
        const unsigned cap = 32u, miss = cnt[i] < cap ? cap - cnt[i] : 0u;
        if (role < 0 && k < miss) role = i * 32 + cnt[i] + k;
        if (role < 0) k -= miss;
      }
    }
    *(LAS int*)c.lds = role;
  }
  __syncthreads();
  const int role = __builtin_amdgcn_readfirstlane(*(LAS int*)c.lds);
  c.mb = mbx_base(ws); c.flip = 8192;
  __syncthreads();
  if (role < 0) return;
  c.role = role;
  MS(0);
  const int x = role >> 5, t = role & 31;
  const int h = 8 * (x - 4) + (t >> 2), kind = t & 3;
  if (x >= 4 && kind == 3) { mod_part(c, (h & 7) * 4 + (h >> 3), karg(A_C), karg(A_WMOD), ctl); MS(5); }
  if (x >= 4 ? kind == 0 : t >= 8) pre_work(c, x >= 4 ? h : 8 * x + (t - 8) / 3, x >= 4 ? 3 : (t - 8) % 3, karg(A_WV), karg(A_WO), ws);
  if (x < 4) {
    if (t < 8) role_A(c, 8 * x + t, karg(A_WQ), karg(A_WK));
    else if (t < 28) role_B(c, 20 * x + t - 8);
    else { if (c.wave < 2) probe_vote(ws, 2 * (4 * x + t - 28) + c.wave); role_P(c, 4 * x + t - 28); }
  } else if (kind == 3) role_O(c, h, karg(A_WO), ctl);
  else {
    role_G(c, h, kind, karg(A_WQ + kind), karg(A_X), karg(A_MEM0), ctl);
    if (kind == 0) role_N(c, x - 4, t >> 2, karg(A_X), ws);
    else if (kind == 1) role_M2(c, x - 4, t >> 2, karg(A_W2), karg(A_B2), ws, (u16*)karg(A_OUT));
    else role_M1(c, x - 4, t >> 2, karg(A_W1), karg(A_B1));
  }
  MS(7);
}

}

static int g_par[8] = {146, 80, 10, 100, 12, 13, 30, 55};

extern "C" void kernel_launch(void* const* d_in, const int* in_sizes, int n_in, void* d_out, int out_size, void* d_ws, size_t ws_size,
                              hipStream_t stream) {
  KArgs ka;
  const int src[12] = {0, 1, 12, 13, 2, 3, 4, 5, 8, 9, 10, 11};
  for (int i = 0; i < 12; ++i) ka.p[i] = (const u16*)d_in[src[i]];
  ka.p[12] = (const u16*)d_in[0];
  ka.out = (u16*)d_out; ka.ws = (unsigned char*)d_ws;
  for (int i = 0; i < 8; ++i) ka.par[i] = g_par[i];
  (void)hipFuncSetAttribute((const void*)k_rec, hipFuncAttributeMaxDynamicSharedMemorySize, REC_LDS);
  k_init<<<CT_WORDS / 1024, 256, 0, stream>>>(ka.ws);
  k_rec<<<256, 256, REC_LDS, stream>>>(ka);
}
```
